```python
import torch

def _softmax(x, axis=-1):
    return torch.softmax(x, dim=axis)
def _gelu(x):
    return torch.nn.functional.gelu(x, approximate="tanh")
def _silu(x):
    return torch.nn.functional.silu(x)
def _sigmoid(x):
    return torch.sigmoid(x)
def _layernorm(x, axis=-1, eps=1e-5):
    m = x.mean(dim=axis, keepdim=True)
    v = x.var(dim=axis, keepdim=True, unbiased=False)
    return (x - m) / torch.sqrt(v + eps)
def _scatter_add(tgt, idx, src, axis=0):
    return tgt.clone().index_add_(axis, idx, src)
def _segment_sum(src, seg, n):
    out = torch.zeros((n,) + src.shape[1:], dtype=src.dtype)
    return out.index_add_(0, seg, src)
def _pad(x, pw):
    flat = tuple(v for pair in reversed(pw) for v in pair)
    return torch.nn.functional.pad(x, flat)

kernel_name = 'sequential_preLN__ssm_conv_h3__mixer_channel_mlp__prompt'

def setup_inputs():

    _w = (lambda a: (lambda t: t.to(torch.bfloat16) if t.is_floating_point() else t)(torch.as_tensor(a)))
    def _A_setup():
        B, S, D = 1, 2048, 512
        ks = 4
        return {
            "q": _w((lambda *s: torch.randn(*s, dtype=torch.bfloat16) * 0.05)(B, S, D)),
            "k": _w((lambda *s: torch.randn(*s, dtype=torch.bfloat16) * 0.2)(B, S, D)),
            "v": _w((lambda *s: torch.randn(*s, dtype=torch.bfloat16) * 0.2)(B, S, D)),
            "shift_k": _w((lambda *s: torch.randn(*s, dtype=torch.bfloat16) * 0.2)(D, ks) * 0.5),
            "ssm_k": _w((lambda *s: torch.randn(*s, dtype=torch.bfloat16) * 0.8)(D, S) * (1.0 / S ** 0.5)),
            "ssm_D": _w((lambda *s: torch.randn(*s, dtype=torch.bfloat16) * 0.2)(D)),
        }
    _A_in = _A_setup()

    def _B_setup():
        B, S, D, F = 1, 2048, 512, 1792
        return {
            "x": _w((lambda *s: torch.randn(*s, dtype=torch.bfloat16) * 0.05)(B, S, D)),
            "w1": _w((lambda *s: torch.randn(*s, dtype=torch.bfloat16) * 0.8)(D, F) * (D ** -0.5)),
            "b1": _w((lambda *s: torch.randn(*s, dtype=torch.bfloat16) * 0.4)(F)),
            "w2": _w((lambda *s: torch.randn(*s, dtype=torch.bfloat16) * 0.05)(F, D) * (F ** -0.5)),
            "b2": _w((lambda *s: torch.randn(*s, dtype=torch.bfloat16) * 0.05)(D)),
        }
    _B_in = _B_setup()

    return {
        "A_q": _A_in["q"],
        "A_k": _A_in["k"],
        "A_v": _A_in["v"],
        "A_shift_k": _A_in["shift_k"],
        "A_ssm_k": _A_in["ssm_k"],
        "A_ssm_D": _A_in["ssm_D"],
        "B_x": _B_in["x"],
        "B_w1": _B_in["w1"],
        "B_b1": _B_in["b1"],
        "B_w2": _B_in["w2"],
        "B_b2": _B_in["b2"],
    }

def reference(A_q, A_k, A_v, A_shift_k, A_ssm_k, A_ssm_D, B_x, B_w1, B_b1, B_w2, B_b2):
    def _A_ref(q, k, v, shift_k, ssm_k, ssm_D):
        B, S, D = q.shape
        ks = shift_k.shape[-1]
        k_pad = _pad(k, ((0, 0), (ks - 1, 0), (0, 0)))
        k_shift = torch.zeros_like(k)
        for t in range(ks):
            k_shift = k_shift + k_pad[:, t:t + S, :] * shift_k[:, ks - 1 - t][None, None, :]
        kv = k_shift * v
        L = 2 * S
        kv_pad = _pad(kv, ((0, 0), (0, S), (0, 0)))
        kf_pad = _pad(ssm_k, ((0, 0), (0, S)))
        kv_f = (lambda x, axes=(-2,-1): torch.fft.rfft2(x.float(), dim=axes))(kv_pad[:, None, :, :], axes=(1, 2))
        kf_f = (lambda x, axes=(-2,-1): torch.fft.rfft2(x.float(), dim=axes))(kf_pad[:, None, :], axes=(1, 2))
        kf_f = (lambda x, *d: x.permute(*d))(kf_f, 1, 2, 0)
        y_f = kv_f * kf_f[None, :, :, :]
        y = (lambda x, s, axes=(-2,-1): torch.fft.irfft2(x, s=s, dim=axes).to(torch.bfloat16))(y_f, (1, L), axes=(1, 2))[:, 0, :S, :]
        y = y + kv * ssm_D[None, None, :]
        return q * y

    def _B_ref(x, w1, b1, w2, b2):
        y = _layernorm(x, axis=-1)
        h = _gelu(torch.einsum("bsd,df->bsf", y, w1) + b1)
        return torch.einsum("bsf,fd->bsd", h, w2) + b2

    x = A_q
    h = x + _A_ref(_layernorm(x, axis=-1), A_k, A_v, A_shift_k, A_ssm_k, A_ssm_D)
    h2 = h + _B_ref(_layernorm(h, axis=-1), B_w1, B_b1, B_w2, B_b2)
    return h2
```

```cpp
#include <hip/hip_runtime.h>
#include <hip/hip_bf16.h>

#define GAS __attribute__((address_space(1)))
#define LAS __attribute__((address_space(3)))
typedef unsigned short u16;
typedef unsigned v4u __attribute__((ext_vector_type(4)));
typedef unsigned v2u __attribute__((ext_vector_type(2)));
typedef float f32x4 __attribute__((ext_vector_type(4)));
typedef float f32x2 __attribute__((ext_vector_type(2)));
typedef short s16x4 __attribute__((ext_vector_type(4)));
typedef short bf16x8 __attribute__((ext_vector_type(8)));
typedef __bf16 bf2 __attribute__((ext_vector_type(2)));

constexpr int S = 2048, D = 512, F = 1792;
constexpr size_t OFF_XR = 0;
constexpr size_t OFF_XT = OFF_XR + (size_t)D * S * 2;
constexpr size_t OFF_HT = OFF_XT + (size_t)D * S * 2;
constexpr size_t OFF_ST = OFF_HT + (size_t)D * S * 2;
constexpr size_t OFF_W1 = OFF_ST + 16384;
constexpr size_t OFF_W2 = OFF_W1 + (size_t)D * F * 2;
constexpr size_t OFF_HF = OFF_W2 + (size_t)D * F * 2;
constexpr size_t WS_END = OFF_HF + (size_t)S * F * 2;

struct Params {
  const u16 *q, *k, *v, *shk, *ssk, *ssd, *w1, *b1, *w2, *b2;
  u16* out;
  unsigned char* ws;
};

__device__ __forceinline__ float bflo(unsigned u) { return __builtin_bit_cast(float, u << 16); }
__device__ __forceinline__ float bfhi(unsigned u) { return __builtin_bit_cast(float, u & 0xffff0000u); }
__device__ __forceinline__ unsigned pk(float lo, float hi) {
  f32x2 f = {lo, hi};
  return __builtin_bit_cast(unsigned, __builtin_convertvector(f, bf2));
}
__device__ __forceinline__ float rbf(float x) { return bflo(pk(x, x)); }
__device__ __forceinline__ f32x4 mfma(v4u a, v4u b, f32x4 c) {
  return __builtin_amdgcn_mfma_f32_16x16x32_bf16(__builtin_bit_cast(bf16x8, a), __builtin_bit_cast(bf16x8, b), c, 0, 0, 0);
}
__device__ __forceinline__ v2u trread(LAS unsigned char* p) {
  return __builtin_bit_cast(v2u, __builtin_amdgcn_ds_read_tr16_b64_v4i16((LAS s16x4*)p));
}
__device__ __forceinline__ float dot2(unsigned a, unsigned b, float c) {
  return __builtin_amdgcn_fdot2_f32_bf16(__builtin_bit_cast(bf2, a), __builtin_bit_cast(bf2, b), c, false);
}
__device__ __forceinline__ void glds16(const GAS void* g, LAS void* l) {
  __builtin_amdgcn_global_load_lds(g, l, 16, 0, 0);
}
#define WAIT_V(n) asm volatile("s_waitcnt vmcnt(" #n ")" ::: "memory")
#define WAIT_L0() asm volatile("s_waitcnt lgkmcnt(0)" ::: "memory")
#define BAR() __builtin_amdgcn_s_barrier()
__device__ __forceinline__ float shx(float v, int m) { return __shfl_xor(v, m); }

constexpr int A_ROW = 144;
constexpr int A_K = 0, A_V = A_K + 68 * A_ROW, A_Q = A_V + 64 * A_ROW, A_W = A_Q + 64 * A_ROW;
constexpr int A_WROW = 272;
constexpr int A_W2 = A_W + 32 * A_WROW, A_LDS = A_W2 + 32 * A_WROW;

__device__ __forceinline__ void phase_a(const Params& p, LAS unsigned char* L, int bid) {
  const int tid = threadIdx.x, lane = tid & 63, w = __builtin_amdgcn_readfirstlane(tid >> 6);
  const int b = bid & 7, a = bid >> 3;
  const int r = tid >> 3, c = tid & 7;
  const size_t g0 = (size_t)(64 * a + r) * D + 64 * b + 8 * c;
  const v4u kk = *(const GAS v4u*)(p.k + g0);
  const v4u vv = *(const GAS v4u*)(p.v + g0);
  const v4u qq = *(const GAS v4u*)(p.q + g0);
  v4u kh = {0, 0, 0, 0};
  if (tid < 32 && a > 0) kh = *(const GAS v4u*)(p.k + (size_t)(64 * a - 4 + r) * D + 64 * b + 8 * c);
  const int srow = 64 * a + 8 * b + w;
  const v4u qs = *(const GAS v4u*)(p.q + (size_t)srow * D + 8 * lane);
  v4u w1v = {0, 0, 0, 0}, w2v = {0, 0, 0, 0};
  const int wr = tid >> 4, wc = tid & 15;
  if (bid < 224) {
    w1v = *(const GAS v4u*)(p.w1 + (size_t)(32 * (bid / 14) + wr) * F + 128 * (bid % 14) + 8 * wc);
    w2v = *(const GAS v4u*)(p.w2 + (size_t)(32 * (bid >> 2) + wr) * D + 128 * (bid & 3) + 8 * wc);
  }
  const int g = lane >> 4, i = lane & 15, tq = i >> 2, tp = i & 3;
  const int ch = 64 * b + 16 * (w & 3) + i, seg = 4 * (w >> 2) + g;
  const v2u shw = *(const GAS v2u*)(p.shk + 4 * ch);
  *(LAS v4u*)(L + A_K + (r + 4) * A_ROW + 16 * c) = kk;
  *(LAS v4u*)(L + A_V + r * A_ROW + 16 * c) = vv;
  *(LAS v4u*)(L + A_Q + r * A_ROW + 16 * c) = qq;
  if (tid < 32) *(LAS v4u*)(L + A_K + r * A_ROW + 16 * c) = kh;
  *(LAS v4u*)(L + A_W + wr * A_WROW + 16 * wc) = w1v;
  *(LAS v4u*)(L + A_W2 + wr * A_WROW + 16 * wc) = w2v;
  {
    float x[8] = {bflo(qs.x), bfhi(qs.x), bflo(qs.y), bfhi(qs.y), bflo(qs.z), bfhi(qs.z), bflo(qs.w), bfhi(qs.w)};
    float s = 0.f;
#pragma unroll
    for (int j = 0; j < 8; ++j) s += x[j];
#pragma unroll
    for (int m = 32; m >= 1; m >>= 1) s += shx(s, m);
    const float mean = s * (1.0f / D);
    float v = 0.f;
#pragma unroll
    for (int j = 0; j < 8; ++j) v += (x[j] - mean) * (x[j] - mean);
#pragma unroll
    for (int m = 32; m >= 1; m >>= 1) v += shx(v, m);
    const float var = rbf(v * (1.0f / D));
    const float sd = __builtin_sqrtf(rbf(var + 1e-5f));
    if (lane == 0) *(GAS unsigned*)(p.ws + OFF_ST + 4 * srow) = pk(mean, sd);
  }
  __syncthreads();
  {
    const int cb = 32 * (w & 3) + 8 * tp;
    LAS unsigned char* kb = L + A_K + (8 * seg + tq) * A_ROW + cb;
    const v2u k0 = trread(kb), k1 = trread(kb + 4 * A_ROW), k2 = trread(kb + 8 * A_ROW);
    LAS unsigned char* vb = L + A_V + (8 * seg + tq) * A_ROW + cb;
    const v2u v0 = trread(vb), v1 = trread(vb + 4 * A_ROW);
    LAS unsigned char* qb = L + A_Q + (8 * seg + tq) * A_ROW + cb;
    const v2u q0 = trread(qb), q1 = trread(qb + 4 * A_ROW);
    const float kf[12] = {bflo(k0.x), bfhi(k0.x), bflo(k0.y), bfhi(k0.y), bflo(k1.x), bfhi(k1.x),
                          bflo(k1.y), bfhi(k1.y), bflo(k2.x), bfhi(k2.x), bflo(k2.y), bfhi(k2.y)};
    const float vf[8] = {bflo(v0.x), bfhi(v0.x), bflo(v0.y), bfhi(v0.y), bflo(v1.x), bfhi(v1.x), bflo(v1.y), bfhi(v1.y)};
    const float w0 = bflo(shw.x), w1 = bfhi(shw.x), w2 = bflo(shw.y), w3 = bfhi(shw.y);
    float kv[8];
#pragma unroll
    for (int j = 0; j < 8; ++j) {
      float t = kf[j + 1] * w3;
      t = __builtin_fmaf(kf[j + 2], w2, t);
      t = __builtin_fmaf(kf[j + 3], w1, t);
      t = __builtin_fmaf(kf[j + 4], w0, t);
      kv[j] = t * vf[j];
    }
    const v4u xr = {pk(kv[7], kv[6]), pk(kv[5], kv[4]), pk(kv[3], kv[2]), pk(kv[1], kv[0])};
    const int s0 = 64 * a + 8 * seg;
    *(GAS v4u*)(p.ws + OFF_XR + ((size_t)ch * S + (S - 8 - s0)) * 2) = xr;
    const v4u xt = {q0.x, q0.y, q1.x, q1.y};
    *(GAS v4u*)(p.ws + OFF_XT + ((size_t)ch * S + s0) * 2) = xt;
  }
  if (bid < 224) {
    {
      const int nq = w >> 1, nb = w & 1;
      LAS unsigned char* wb = L + A_W + (8 * g + tq) * A_WROW + 2 * (32 * nq + 8 * tp + 4 * nb);
      const v2u f0 = trread(wb), f1 = trread(wb + 4 * A_WROW);
      const int ksg = bid / 14, nt = bid % 14, kh2 = ksg >> 3, ks = ksg & 7;
      const v4u f = {f0.x, f0.y, f1.x, f1.y};
      *(GAS v4u*)(p.ws + OFF_W1 + (size_t)(((nt * 8 + kh2 * 4 + nq) * 16) + ks * 2 + nb) * 1024 + lane * 16) = f;
    }
    {
      const int dl = w >> 2, nh = (w >> 1) & 1, db = w & 1;
      LAS unsigned char* wb = L + A_W2 + (8 * g + tq) * A_WROW + 2 * (64 * dl + 32 * nh + 8 * tp + 4 * db);
      const v2u f0 = trread(wb), f1 = trread(wb + 4 * A_WROW);
      const int ksg = bid >> 2, dt = 2 * (bid & 3) + dl, kq = ksg / 14, ks = ksg % 14;
      const v4u f = {f0.x, f0.y, f1.x, f1.y};
      *(GAS v4u*)(p.ws + OFF_W2 + (size_t)(((dt * 8 + kq * 2 + nh) * 28) + ks * 2 + db) * 1024 + lane * 16) = f;
    }
  }
}

constexpr int B_HCS = 4160, B_XCS = 4640;
constexpr int B_HC = 0, B_XC = B_HC + 8 * B_HCS, B_RED = B_XC + 4 * B_XCS, B_LDS = B_RED + 65536;
static_assert(B_RED % 16 == 0, "");

__device__ __forceinline__ void phase_b(const Params& p, LAS unsigned char* L, int bid) {
  const int tid = threadIdx.x, lane = tid & 63, w = __builtin_amdgcn_readfirstlane(tid >> 6);
  const int j = (bid & 7) * 32 + (bid >> 3);
  const int chl = w >> 2, q4 = w & 3, ch = 2 * j + chl;
  {
    const u16* hrow = p.ssk + (size_t)ch * S;
    LAS unsigned char* hc = L + B_HC + w * B_HCS;
#pragma unroll
    for (int i = 0; i < 4; ++i) {
      int idx = 8 * (4 + 64 * i + lane) + 2 * q4 - 30;
      idx = idx > S - 8 ? S - 8 : idx;
      glds16((const GAS void*)(hrow + idx), hc + 64 + 1024 * i);
    }
    if (lane < 32) {
      const int idx = lane + 2 * q4 - 30;
      u16 val = 0;
      if (idx >= 0) val = *(const GAS u16*)(hrow + idx);
      *(LAS u16*)(hc + 2 * lane) = val;
    }
  }
  const int xch = tid >> 8, xc = tid & 255;
  {
    const GAS unsigned char* xr = (const GAS unsigned char*)(p.ws + OFF_XR) + ((size_t)(2 * j + xch) * S + 8 * xc) * 2;
    const v4u x1 = *(const GAS v4u*)xr;
    unsigned nx = 0;
    if (xc < 255) nx = *(const GAS u16*)(xr + 16);
    const v4u x0 = {__builtin_amdgcn_alignbit(x1.y, x1.x, 16), __builtin_amdgcn_alignbit(x1.z, x1.y, 16),
                    __builtin_amdgcn_alignbit(x1.w, x1.z, 16), __builtin_amdgcn_alignbit(nx, x1.w, 16)};
    LAS unsigned char* xb = L + B_XC + xch * 2 * B_XCS + 16 * xc;
    *(LAS v4u*)xb = x0;
    *(LAS v4u*)(xb + B_XCS) = x1;
    if (tid < 128) *(LAS v4u*)(L + B_XC + (tid >> 5) * B_XCS + 4096 + 16 * (tid & 31)) = v4u{0, 0, 0, 0};
  }
  const int c = lane & 15, g = lane >> 4, al = c & 1;
  const int tile = 2 * q4 + al;
  const int s0 = 256 * tile + 8 * g + 32 * (c >> 1);
  const v4u xt = *(const GAS v4u*)(p.ws + OFF_XT + ((size_t)ch * S + s0) * 2);
  const v4u st0 = *(const GAS v4u*)(p.ws + OFF_ST + 4 * s0);
  const v4u st1 = *(const GAS v4u*)(p.ws + OFF_ST + 4 * s0 + 16);
  const float dch = bflo(*(const GAS u16*)(p.ssd + ch));
  WAIT_V(0);
  __syncthreads();

  f32x4 acc[8];
#pragma unroll
  for (int i = 0; i < 8; ++i) acc[i] = f32x4{0.f, 0.f, 0.f, 0.f};
  LAS unsigned char* ab = L + B_HC + (chl * 4 + (c & 3)) * B_HCS + 2 * (224 + 8 * (c >> 2) + 8 * g);
  LAS unsigned char* bb = L + B_XC + (chl * 2 + al) * B_XCS + 2 * (2240 - 1792 - 32 * (c >> 1) + 8 * g);
#pragma unroll
  for (int pp = 0; pp < 2; ++pp) {
    const int cls = 2 * q4 + pp;
    LAS unsigned char* a0 = ab - 64 * cls;
    LAS unsigned char* b0 = bb - 64 * cls;
    v4u af[8], bf[8];
#pragma unroll
    for (int i = 0; i < 8; ++i) af[i] = *(LAS v4u*)(a0 + 512 * i);
#pragma unroll
    for (int i = 0; i < 8; ++i) bf[i] = *(LAS v4u*)(b0 + 512 * (7 - i));
#pragma unroll
    for (int ip = 0; ip < 8; ++ip)
#pragma unroll
      for (int i = ip; i < 8; ++i) acc[i] = mfma(af[i - ip], bf[ip], acc[i]);
  }
  LAS f32x4* red = (LAS f32x4*)(L + B_RED);
#pragma unroll
  for (int i = 0; i < 8; ++i)
    if ((i >> 1) != q4) red[(w * 8 + i) * 64 + lane] = acc[i];
  __syncthreads();
  f32x4 ya = {0.f, 0.f, 0.f, 0.f}, yb = {0.f, 0.f, 0.f, 0.f};
#pragma unroll
  for (int i = 0; i < 8; ++i)
    if ((i >> 1) == q4) { if (i & 1) yb = acc[i]; else ya = acc[i]; }
#pragma unroll
  for (int o = 1; o < 4; ++o) {
    const int ww = chl * 4 + ((q4 + o) & 3);
    ya += red[(ww * 8 + 2 * q4) * 64 + lane];
    yb += red[(ww * 8 + 2 * q4 + 1) * 64 + lane];
  }
  float y[8];
#pragma unroll
  for (int r = 0; r < 4; ++r) {
    const float snd = al ? ya[r] : yb[r];
    const float rcv = shx(snd, 1);
    y[2 * r] = al ? rcv : ya[r];
    y[2 * r + 1] = al ? yb[r] : rcv;
  }
  const v4u kvr = *(LAS v4u*)(L + B_XC + (chl * 2 + 1) * B_XCS + 2 * (S - 8 - s0));
  const float kv[8] = {bfhi(kvr.w), bflo(kvr.w), bfhi(kvr.z), bflo(kvr.z), bfhi(kvr.y), bflo(kvr.y), bfhi(kvr.x), bflo(kvr.x)};
  const float x[8] = {bflo(xt.x), bfhi(xt.x), bflo(xt.y), bfhi(xt.y), bflo(xt.z), bfhi(xt.z), bflo(xt.w), bfhi(xt.w)};
  const unsigned st[8] = {st0.x, st0.y, st0.z, st0.w, st1.x, st1.y, st1.z, st1.w};
  float h[8];
#pragma unroll
  for (int e = 0; e < 8; ++e) {
    const float yy = rbf(rbf(y[e]) + rbf(kv[e] * dch));
    const float qn = rbf(rbf(x[e] - bflo(st[e])) * __builtin_amdgcn_rcpf(bfhi(st[e])));
    h[e] = x[e] + rbf(qn * yy);
  }
  const v4u ho = {pk(h[0], h[1]), pk(h[2], h[3]), pk(h[4], h[5]), pk(h[6], h[7])};
  *(GAS v4u*)(p.ws + OFF_HT + ((size_t)ch * S + s0) * 2) = ho;
}

constexpr int D_TILE = 0, D_STAT = 131072, D_CS = D_STAT + 2 * 128 * 8, D_LDS = D_CS + 2 * 128 * 4;

__device__ __forceinline__ void phase_d(const Params& p, LAS unsigned char* L, int bid) {
  const int tid = threadIdx.x, lane = tid & 63, w = __builtin_amdgcn_readfirstlane(tid >> 6);
  const int idx = bid >> 3;
  if (idx >= 28) return;
  const int mt = 2 * (bid & 7) + idx / 14, nt = idx % 14;
  const int kh = w >> 2, nq = w & 3;
  const int g = lane >> 4, i16 = lane & 15;
  {
    const int rl = lane >> 4, sl = lane & 15;
#pragma unroll
    for (int c = 0; c < 4; ++c)
#pragma unroll
      for (int pc = 0; pc < 4; ++pc) {
        const int row0 = 256 * (w >> 2) + 64 * c + 16 * (w & 3) + 4 * pc;
        const int row = row0 + rl;
        const int f = (row & 3) | (((row >> 3) & 1) << 2);
        const GAS unsigned char* src = (const GAS unsigned char*)(p.ws + OFF_HT) + ((size_t)row * S + 128 * mt) * 2 + 32 * ((sl >> 1) ^ f) + 16 * (sl & 1);
        glds16(src, L + D_TILE + row0 * 256);
      }
  }
  v4u wf[16];
  {
    const GAS v4u* wp = (const GAS v4u*)(p.ws + OFF_W1) + (size_t)(nt * 8 + w) * 16 * 64 + lane;
#pragma unroll
    for (int i = 0; i < 16; ++i) wf[i] = wp[i * 64];
  }
  const v4u b1v = *(const GAS v4u*)(p.b1 + 128 * nt + 32 * nq + 8 * g);
  {
    float cs[2] = {0.f, 0.f};
#pragma unroll
    for (int i = 0; i < 16; ++i) {
      float t = cs[i & 1];
      t = dot2(wf[i].x, 0x3f803f80u, t); t = dot2(wf[i].y, 0x3f803f80u, t);
      t = dot2(wf[i].z, 0x3f803f80u, t); t = dot2(wf[i].w, 0x3f803f80u, t);
      cs[i & 1] = t;
    }
#pragma unroll
    for (int nb = 0; nb < 2; ++nb) {
      float t = cs[nb];
      t += shx(t, 16); t += shx(t, 32);
      if (lane < 16) *(LAS float*)(L + D_CS + ((kh * 4 + nq) * 32 + nb * 16 + lane) * 4) = t;
    }
  }
  f32x4 acc[8][2];
#pragma unroll
  for (int r = 0; r < 8; ++r) { acc[r][0] = f32x4{0.f, 0.f, 0.f, 0.f}; acc[r][1] = f32x4{0.f, 0.f, 0.f, 0.f}; }
  float s1[2] = {0.f, 0.f}, s2[2] = {0.f, 0.f};
  const int tq = i16 >> 2, tp = i16 & 3;
  const int fl = tq | ((g & 1) << 2);
  const unsigned basel = (unsigned)(D_TILE + 256 * (256 * kh + 8 * g + tq) + 32 * fl + 8 * tp);
  WAIT_V(0);
  BAR();
#pragma unroll
  for (int ks = 0; ks < 8; ++ks) {
#pragma unroll
    for (int rb = 0; rb < 8; ++rb) {
      LAS unsigned char* ad = L + ((basel ^ (32u * rb)) + 8192 * ks);
      const v2u f0 = trread(ad), f1 = trread(ad + 1024);
      const v4u hf = {f0.x, f0.y, f1.x, f1.y};
      acc[rb][0] = mfma(wf[2 * ks], hf, acc[rb][0]);
      acc[rb][1] = mfma(wf[2 * ks + 1], hf, acc[rb][1]);
      if ((rb >> 1) == nq) {
        float a = s1[rb & 1], b = s2[rb & 1];
        a = dot2(hf.x, 0x3f803f80u, a); b = dot2(hf.x, hf.x, b);
        a = dot2(hf.y, 0x3f803f80u, a); b = dot2(hf.y, hf.y, b);
        a = dot2(hf.z, 0x3f803f80u, a); b = dot2(hf.z, hf.z, b);
        a = dot2(hf.w, 0x3f803f80u, a); b = dot2(hf.w, hf.w, b);
        s1[rb & 1] = a; s2[rb & 1] = b;
      }
    }
  }
#pragma unroll
  for (int r = 0; r < 2; ++r) {
    float a = s1[r], b = s2[r];
    a += shx(a, 16); a += shx(a, 32);
    b += shx(b, 16); b += shx(b, 32);
    if (lane < 16) *(LAS f32x2*)(L + D_STAT + (kh * 128 + (2 * nq + r) * 16 + lane) * 8) = f32x2{a, b};
  }
  __syncthreads();
  LAS f32x4* red = (LAS f32x4*)(L + D_TILE);
#pragma unroll
  for (int r = 0; r < 8; ++r)
    if ((r >> 2) != kh) {
      red[((w * 4 + (r & 3)) * 2 + 0) * 64 + lane] = acc[r][0];
      red[((w * 4 + (r & 3)) * 2 + 1) * 64 + lane] = acc[r][1];
    }
  __syncthreads();
  const int pw = w ^ 4;
  f32x4 cs0, cs1;
  {
    LAS f32x4* c4 = (LAS f32x4*)(L + D_CS);
    cs0 = c4[(nq * 32 + 0 + 4 * g) / 4] + c4[((4 + nq) * 32 + 0 + 4 * g) / 4];
    cs1 = c4[(nq * 32 + 16 + 4 * g) / 4] + c4[((4 + nq) * 32 + 16 + 4 * g) / 4];
  }
  const float b1f[8] = {bflo(b1v.x), bfhi(b1v.x), bflo(b1v.y), bfhi(b1v.y), bflo(b1v.z), bfhi(b1v.z), bflo(b1v.w), bfhi(b1v.w)};
#pragma unroll
  for (int r4 = 0; r4 < 4; ++r4) {
    f32x4 a0, a1;
#pragma unroll
    for (int r = 0; r < 8; ++r)
      if ((r >> 2) == kh && (r & 3) == r4) { a0 = acc[r][0]; a1 = acc[r][1]; }
    a0 += red[((pw * 4 + r4) * 2 + 0) * 64 + lane];
    a1 += red[((pw * 4 + r4) * 2 + 1) * 64 + lane];
    const int rb = 4 * kh + r4;
    const f32x2 sa = *(LAS f32x2*)(L + D_STAT + (rb * 16 + i16) * 8), sb = *(LAS f32x2*)(L + D_STAT + (128 + rb * 16 + i16) * 8);
    const float mean = (sa.x + sb.x) * (1.0f / D);
    float var = (sa.y + sb.y) * (1.0f / D) - mean * mean;
    var = var > 0.f ? var : 0.f;
    const float r1 = __builtin_amdgcn_rsqf(var + 1e-5f);
    const float var2 = var * r1 * r1;
    const float rs = r1 * __builtin_amdgcn_rsqf(var2 + 1e-5f);
    float o[8];
#pragma unroll
    for (int e = 0; e < 8; ++e) {
      const float a = e < 4 ? a0[e & 3] : a1[e & 3];
      const float csn = e < 4 ? cs0[e & 3] : cs1[e & 3];
      const float u = rbf(rs * (a - mean * csn)) + b1f[e];
      const float x = rbf(u);
      const float z = 1.5957691216057308f * x * __builtin_fmaf(0.044715f * x, x, 1.0f);
      const float ex = __builtin_amdgcn_exp2f(-1.4426950408889634f * z);
      o[e] = x * __builtin_amdgcn_rcpf(1.0f + ex);
    }
    const v4u ov = {pk(o[0], o[1]), pk(o[2], o[3]), pk(o[4], o[5]), pk(o[6], o[7])};
    const int rbg = 8 * mt + rb;
    *(GAS v4u*)(p.ws + OFF_HF + (size_t)(((rbg >> 2) * 56 + 4 * nt + nq) * 4 + (rbg & 3)) * 1024 + lane * 16) = ov;
  }
}

constexpr int E_SLOTS = 9;
constexpr int E_RING = 0, E_RES = E_SLOTS * 16384, E_LDS = E_RES + 8192;

__device__ __forceinline__ void phase_e(const Params& p, LAS unsigned char* L, int bid) {
  const int tid = threadIdx.x, lane = tid & 63, w = __builtin_amdgcn_readfirstlane(tid >> 6);
  const int idx = bid >> 3, e = 4 * (bid & 7) + (idx >> 3), dt = idx & 7;
  const int kq = w >> 1, nh = w & 1;
  const int g = lane >> 4, i16 = lane & 15;
  const GAS unsigned char* hsrc = (const GAS unsigned char*)(p.ws + OFF_HF) + (size_t)((e * 56 + 14 * kq) * 4 + 2 * nh) * 1024 + lane * 16;
  auto issue = [&](int c) {
    LAS unsigned char* dst = L + E_RING + (c % E_SLOTS) * 16384 + (kq * 4 + 2 * nh) * 1024;
    glds16(hsrc + (size_t)c * 4096, dst);
    glds16(hsrc + (size_t)c * 4096 + 1024, dst + 1024);
  };
  {
    const int row = 8 * w + (lane >> 3);
    glds16((const GAS unsigned char*)(p.ws + OFF_HT) + ((size_t)(64 * dt + row) * S + 64 * e) * 2 + 16 * (lane & 7), L + E_RES + w * 1024);
  }
  v4u wf[28];
  {
    const GAS v4u* wp = (const GAS v4u*)(p.ws + OFF_W2) + (size_t)(dt * 8 + w) * 28 * 64 + lane;
#pragma unroll
    for (int i = 0; i < 28; ++i) wf[i] = wp[i * 64];
  }
  const v4u b2v = *(const GAS v4u*)(p.b2 + 64 * dt + 32 * nh + 8 * g);
  __builtin_amdgcn_sched_barrier(0);
#pragma unroll
  for (int c = 0; c < E_SLOTS; ++c) issue(c);
  f32x4 acc[4][2];
#pragma unroll
  for (int r = 0; r < 4; ++r) { acc[r][0] = f32x4{0.f, 0.f, 0.f, 0.f}; acc[r][1] = f32x4{0.f, 0.f, 0.f, 0.f}; }
#pragma unroll
  for (int c = 0; c < 14; ++c) {
    if (c == 0) { WAIT_V(16); }
    else if (c <= 6) { WAIT_V(14); }
    else if (c == 7) { WAIT_V(12); }
    else if (c == 8) { WAIT_V(10); }
    else if (c == 9) { WAIT_V(8); }
    else if (c == 10) { WAIT_V(6); }
    else if (c == 11) { WAIT_V(4); }
    else if (c == 12) { WAIT_V(2); }
    else { WAIT_V(0); }
    WAIT_L0();
    BAR();
    if (c >= 1 && c - 1 + E_SLOTS < 14) issue(c - 1 + E_SLOTS);
    LAS v4u* src = (LAS v4u*)(L + E_RING + (c % E_SLOTS) * 16384 + kq * 4096) + lane;
    v4u hf[4];
#pragma unroll
    for (int r = 0; r < 4; ++r) hf[r] = src[r * 64];
#pragma unroll
    for (int r = 0; r < 4; ++r) {
      acc[r][0] = mfma(wf[2 * c], hf[r], acc[r][0]);
      acc[r][1] = mfma(wf[2 * c + 1], hf[r], acc[r][1]);
    }
  }
  __syncthreads();
  LAS f32x4* red = (LAS f32x4*)(L + E_RING);
#pragma unroll
  for (int r = 0; r < 4; ++r)
    if (r != kq) {
      red[((w * 4 + r) * 2 + 0) * 64 + lane] = acc[r][0];
      red[((w * 4 + r) * 2 + 1) * 64 + lane] = acc[r][1];
    }
  __syncthreads();
  f32x4 a0, a1;
#pragma unroll
  for (int r = 0; r < 4; ++r)
    if (r == kq) { a0 = acc[r][0]; a1 = acc[r][1]; }
#pragma unroll
  for (int o = 1; o < 4; ++o) {
    const int ww = ((kq + o) & 3) * 2 + nh;
    a0 += red[((ww * 4 + kq) * 2 + 0) * 64 + lane];
    a1 += red[((ww * 4 + kq) * 2 + 1) * 64 + lane];
  }
  const int tq = i16 >> 2, tp = i16 & 3;
  LAS unsigned char* rp = L + E_RES + (32 * nh + 8 * g + tq) * 128 + 32 * kq + 8 * tp;
  const v2u h0 = trread(rp), h1 = trread(rp + 512);
  const float hv[8] = {bflo(h0.x), bfhi(h0.x), bflo(h0.y), bfhi(h0.y), bflo(h1.x), bfhi(h1.x), bflo(h1.y), bfhi(h1.y)};
  const float b2f[8] = {bflo(b2v.x), bfhi(b2v.x), bflo(b2v.y), bfhi(b2v.y), bflo(b2v.z), bfhi(b2v.z), bflo(b2v.w), bfhi(b2v.w)};
  float o[8];
#pragma unroll
  for (int x = 0; x < 8; ++x) {
    const float a = x < 4 ? a0[x & 3] : a1[x & 3];
    o[x] = hv[x] + rbf(rbf(a) + b2f[x]);
  }
  const v4u ov = {pk(o[0], o[1]), pk(o[2], o[3]), pk(o[4], o[5]), pk(o[6], o[7])};
  *(GAS v4u*)(p.out + (size_t)(64 * e + 16 * kq + i16) * D + 64 * dt + 32 * nh + 8 * g) = ov;
}

__global__ void __launch_bounds__(512, 2) k_a(Params p) {
  __shared__ __attribute__((aligned(16))) unsigned char smem[A_LDS];
  phase_a(p, (LAS unsigned char*)smem, blockIdx.x);
}
__global__ void __launch_bounds__(512, 2) k_b(Params p) {
  __shared__ __attribute__((aligned(16))) unsigned char smem[B_LDS];
  phase_b(p, (LAS unsigned char*)smem, blockIdx.x);
}
__global__ void __launch_bounds__(512, 2) k_d(Params p) {
  __shared__ __attribute__((aligned(16))) unsigned char smem[D_LDS];
  phase_d(p, (LAS unsigned char*)smem, blockIdx.x);
}
__global__ void __launch_bounds__(512, 2) k_e(Params p) {
  __shared__ __attribute__((aligned(16))) unsigned char smem[E_LDS];
  phase_e(p, (LAS unsigned char*)smem, blockIdx.x);
}

extern "C" void kernel_launch(void* const* d_in, const int* in_sizes, int n_in, void* d_out, int out_size, void* d_ws,
                              size_t ws_size, hipStream_t stream) {
  Params p;
  p.q = (const u16*)d_in[0]; p.k = (const u16*)d_in[1]; p.v = (const u16*)d_in[2];
  p.shk = (const u16*)d_in[3]; p.ssk = (const u16*)d_in[4]; p.ssd = (const u16*)d_in[5];
  p.w1 = (const u16*)d_in[7]; p.b1 = (const u16*)d_in[8]; p.w2 = (const u16*)d_in[9]; p.b2 = (const u16*)d_in[10];
  p.out = (u16*)d_out; p.ws = (unsigned char*)d_ws;
  k_a<<<256, 512, 0, stream>>>(p);
  k_b<<<256, 512, 0, stream>>>(p);
  k_d<<<256, 512, 0, stream>>>(p);
  k_e<<<256, 512, 0, stream>>>(p);
}
```

```cpp
#include <hip/hip_runtime.h>
#include <hip/hip_bf16.h>

#define GAS __attribute__((address_space(1)))
#define LAS __attribute__((address_space(3)))
typedef unsigned short u16;
typedef unsigned v4u __attribute__((ext_vector_type(4)));
typedef unsigned v2u __attribute__((ext_vector_type(2)));
typedef float f32x16 __attribute__((ext_vector_type(16)));
typedef float f32x4 __attribute__((ext_vector_type(4)));
typedef float f32x2 __attribute__((ext_vector_type(2)));
typedef short s16x4 __attribute__((ext_vector_type(4)));
typedef short bf16x8 __attribute__((ext_vector_type(8)));
typedef __bf16 bf2 __attribute__((ext_vector_type(2)));
typedef __amdgpu_buffer_rsrc_t rsrc_t;

constexpr int S = 2048, D = 512, F = 1792;
constexpr unsigned OFF_EP = 0;
constexpr unsigned OFF_FA1 = 4096;
constexpr unsigned OFF_FA3 = OFF_FA1 + 8192;
constexpr unsigned OFF_FB = OFF_FA3 + 8192;
constexpr unsigned OFF_FD = OFF_FB + 8192;
constexpr unsigned OFF_XR = 65536;
constexpr unsigned OFF_XT = OFF_XR + D * S * 2;
constexpr unsigned OFF_HT = OFF_XT + D * S * 2;
constexpr unsigned OFF_ST = OFF_HT + D * S * 2;
constexpr unsigned OFF_W1 = OFF_ST + 16384;
constexpr unsigned OFF_W2 = OFF_W1 + D * F * 2;
constexpr unsigned OFF_HF = OFF_W2 + D * F * 2;
constexpr unsigned WS_END = OFF_HF + S * F * 2;
constexpr int SPIN_CAP = 1 << 16;

struct Params {
  const u16 *q, *k, *v, *shk, *ssk, *ssd, *w1, *b1, *w2, *b2;
  u16* out;
  unsigned char* ws;
};

__device__ __forceinline__ float asf(unsigned u) { return __builtin_bit_cast(float, u); }
__device__ __forceinline__ unsigned asu(float f) { return __builtin_bit_cast(unsigned, f); }
__device__ __forceinline__ float bflo(unsigned u) { return asf(u << 16); }
__device__ __forceinline__ float bfhi(unsigned u) { return asf(u & 0xffff0000u); }
__device__ __forceinline__ unsigned pk(float lo, float hi) {
  f32x2 f = {lo, hi};
  return __builtin_bit_cast(unsigned, __builtin_convertvector(f, bf2));
}
__device__ __forceinline__ f32x2 up2(unsigned u) { return f32x2{bflo(u), bfhi(u)}; }
__device__ __forceinline__ f32x2 fma2(f32x2 a, f32x2 b, f32x2 c) { return __builtin_elementwise_fma(a, b, c); }
__device__ __forceinline__ f32x4 mfma16(v4u a, v4u b, f32x4 c) {
  return __builtin_amdgcn_mfma_f32_16x16x32_bf16(__builtin_bit_cast(bf16x8, a), __builtin_bit_cast(bf16x8, b), c, 0, 0, 0);
}
__device__ __forceinline__ f32x16 mfma32(v4u a, v4u b, f32x16 c) {
  return __builtin_amdgcn_mfma_f32_32x32x16_bf16(__builtin_bit_cast(bf16x8, a), __builtin_bit_cast(bf16x8, b), c, 0, 0, 0);
}
__device__ __forceinline__ v2u trread(LAS unsigned char* p) {
  return __builtin_bit_cast(v2u, __builtin_amdgcn_ds_read_tr16_b64_v4i16((LAS s16x4*)p));
}
__device__ __forceinline__ v4u trfrag(LAS unsigned char* p, int second) {
  const v2u a = trread(p), b = trread(p + second);
  return v4u{a.x, a.y, b.x, b.y};
}
__device__ __forceinline__ float dot2(unsigned a, unsigned b, float c) {
  return __builtin_amdgcn_fdot2_f32_bf16(__builtin_bit_cast(bf2, a), __builtin_bit_cast(bf2, b), c, false);
}
constexpr int SC1 = 16;
#define DMA_HEAD "s_mov_b32 m0, %1\n\ts_nop 0\n\t"
#define DMA_P(o, f) "global_load_lds_dwordx4 %0, off offset:" #o f "\n\t"
__device__ __forceinline__ void dma4(const GAS void* g, unsigned lds) {
  asm volatile(DMA_HEAD DMA_P(0, "") DMA_P(1024, "") DMA_P(2048, "") DMA_P(3072, "") :: "v"(g), "s"(lds) : "memory");
}
__device__ __forceinline__ void dma3(const GAS void* g, unsigned lds) {
  asm volatile(DMA_HEAD DMA_P(0, "") DMA_P(1024, "") DMA_P(2048, "") :: "v"(g), "s"(lds) : "memory");
}
__device__ __forceinline__ void dma1(const GAS void* g, unsigned lds) {
  asm volatile(DMA_HEAD DMA_P(0, "") :: "v"(g), "s"(lds) : "memory");
}
__device__ __forceinline__ void dma4s(const GAS void* g, unsigned lds) {
  asm volatile(DMA_HEAD DMA_P(0, " sc1") DMA_P(1024, " sc1") DMA_P(2048, " sc1") DMA_P(3072, " sc1") :: "v"(g), "s"(lds) : "memory");
}
__device__ __forceinline__ void dma3s(const GAS void* g, unsigned lds) {
  asm volatile(DMA_HEAD DMA_P(0, " sc1") DMA_P(1024, " sc1") DMA_P(2048, " sc1") :: "v"(g), "s"(lds) : "memory");
}
__device__ __forceinline__ void dma2s(const GAS void* g, unsigned lds) {
  asm volatile(DMA_HEAD DMA_P(0, " sc1") DMA_P(1024, " sc1") :: "v"(g), "s"(lds) : "memory");
}
__device__ __forceinline__ void dma1s(const GAS void* g, unsigned lds) {
  asm volatile(DMA_HEAD DMA_P(0, " sc1") :: "v"(g), "s"(lds) : "memory");
}
__device__ __forceinline__ v4u ld16(rsrc_t rs, unsigned off) { return __builtin_amdgcn_raw_buffer_load_b128(rs, off, 0, SC1); }
__device__ __forceinline__ void st16(v4u v, rsrc_t rs, unsigned off) { __builtin_amdgcn_raw_buffer_store_b128(v, rs, off, 0, SC1); }
__device__ __forceinline__ void st4(unsigned v, rsrc_t rs, unsigned off) { __builtin_amdgcn_raw_buffer_store_b32(v, rs, off, 0, SC1); }
template <int CTRL, int RM>
__device__ __forceinline__ float dppadd(float x) {
  return x + asf(__builtin_amdgcn_update_dpp(0, asu(x), CTRL, RM, 0xf, true));
}
__device__ __forceinline__ float wavesum(float x) {
  x = dppadd<0x111, 0xf>(x); x = dppadd<0x112, 0xf>(x); x = dppadd<0x114, 0xf>(x); x = dppadd<0x118, 0xf>(x);
  x = dppadd<0x142, 0xa>(x); x = dppadd<0x143, 0xc>(x);
  return x;
}
__device__ __forceinline__ float halfsum(float x) {
  const unsigned u = asu(x);
  auto r = __builtin_amdgcn_permlane32_swap(u, u, false, false);
  const unsigned r0 = r[0], r1 = r[1];
  return asf(r0) + asf(r1);
}
#define WAIT_V(n) asm volatile("s_waitcnt vmcnt(" #n ")" ::: "memory")
#define BARRIER() do { asm volatile("s_waitcnt lgkmcnt(0)" ::: "memory"); __builtin_amdgcn_s_barrier(); asm volatile("" ::: "memory"); } while (0)
#define SCHED() __builtin_amdgcn_sched_barrier(0)
constexpr unsigned ONES = 0x3f803f80u;

#define STAMPT(idx)

constexpr int LDS_BYTES = 163840;
constexpr int A_K = 0, A_V = A_K + 68 * 128, A_Q = A_V + 8192, A_END = A_Q + 8192;
constexpr int B_HCS = 4160, B_XCS = 4640;
constexpr int L_FLAG = LDS_BYTES - 64;
constexpr int B_XC = L_FLAG - 4 * B_XCS, B_HC = B_XC - 8 * B_HCS, B_RED = 0, B_ST = 65536, A_W1 = B_ST + 16384, A_W2 = A_W1 + 8192;
constexpr int D_TILE = 0, D_STAT = 131072, D_CS = D_STAT + 2 * 8 * 64 * 8, D_END = D_CS + 1024;
constexpr int E_RED = 0, E_RES = 143360;
static_assert(A_W2 + 8192 <= B_HC && A_END <= 65536 && B_HC % 16 == 0 && D_END <= E_RES && E_RES + 8192 <= L_FLAG, "");

__device__ __forceinline__ void poll4(rsrc_t rs, unsigned off, unsigned tag) {
  for (int spins = 0; spins < SPIN_CAP; ++spins) {
    const v4u f = ld16(rs, off);
    if (__builtin_amdgcn_ballot_w64(f.x == tag && f.y == tag && f.z == tag && f.w == tag) == ~0ull) break;
    __builtin_amdgcn_s_sleep(1);
    asm volatile("" ::: "memory");
  }
}
__device__ __forceinline__ void poll1(rsrc_t rs, unsigned off, unsigned tag) {
  for (int spins = 0; spins < SPIN_CAP; ++spins) {
    const unsigned f = __builtin_amdgcn_raw_buffer_load_b32(rs, off, 0, SC1);
    if (__builtin_amdgcn_ballot_w64(f == tag) == ~0ull) break;
    __builtin_amdgcn_s_sleep(1);
    asm volatile("" ::: "memory");
  }
}
__device__ __forceinline__ void lds_set(LAS unsigned char* L, int idx) {
  asm volatile("" ::: "memory");
  *(volatile LAS unsigned*)(L + L_FLAG + 4 * idx) = 1u;
}
__device__ __forceinline__ void lds_wait(LAS unsigned char* L, int idx) {
  for (int spins = 0; spins < (SPIN_CAP << 4); ++spins) {
    if (__builtin_amdgcn_readfirstlane(*(volatile LAS unsigned*)(L + L_FLAG + 4 * idx)) != 0) break;
    __builtin_amdgcn_s_sleep(1);
  }
  asm volatile("" ::: "memory");
}
#define GLD(dst, voff, base, imm) asm volatile("global_load_dwordx4 %0, %1, %2 offset:" #imm " sc1" : "=v"(dst) : "v"(voff), "s"(base) : "memory")
#define ALOAD(op, dst, ptr) asm volatile(op " %0, %1, off" : "=v"(dst) : "v"(ptr) : "memory")

__global__ void __launch_bounds__(512, 2) fused(Params p) {
  __shared__ __attribute__((aligned(16))) unsigned char smem[LDS_BYTES];
  LAS unsigned char* L = (LAS unsigned char*)smem;
  const unsigned LB = (unsigned)(size_t)L;
  const int tid = threadIdx.x, lane = tid & 63, w = __builtin_amdgcn_readfirstlane(tid >> 6);
  const int bid = blockIdx.x, xcd = bid & 7, bix = bid >> 3;
  const rsrc_t rs = __builtin_amdgcn_make_buffer_rsrc((void*)p.ws, 0, (int)WS_END, 0x00020000);
  const int g = lane >> 4, i16 = lane & 15, tq = i16 >> 2, tp = i16 & 3;
  const int hh = lane >> 5, blk = g & 1;
  STAMPT(0);

  const int cj = xcd * 32 + bix;
  const bool d_act = bix < 28;
  const int d_mt = 4 * (xcd >> 1) + (d_act ? bix / 7 : 0), d_nt = 7 * (xcd & 1) + bix % 7;
  const int e_mt = 2 * xcd + (bix >> 4), e_j = (bix >> 3) & 1, e_dt = bix & 7;
  const int chl = w >> 2, q4 = w & 3, bch = 2 * cj + chl;

  unsigned tag;
  unsigned hval, dv;
  {
    const int b = xcd, a = bix;
    const int ch = 64 * b + 16 * (w & 3) + i16, seg = 4 * (w >> 2) + g;
    const int srow = 64 * a + 8 * b + w;
    v2u shw; v4u qs;
    {
      const int rl = lane >> 3, sl = lane & 7;
      const int rhok = 4 + 8 * w + rl, rhov = 8 * w + rl;
      const unsigned fk = ((rhok >> 1) & 1) | (((rhok >> 3) & 1) << 1), fv = ((rhov >> 1) & 1) | (((rhov >> 3) & 1) << 1);
      const unsigned rowoff = ((64 * a + 8 * w + rl) * D + 64 * b) * 2 + 16 * (sl & 1);
      const unsigned uk = rowoff + 32 * ((sl >> 1) ^ fk), uv = rowoff + 32 * ((sl >> 1) ^ fv);
      if (w == 0 && a > 0 && lane < 32)
        dma1((const GAS char*)p.k + (((64 * a - 4 + rl) * D + 64 * b) * 2 + 16 * (sl & 1) + 32 * ((sl >> 1) ^ ((rl >> 1) & 1))), LB + A_K);
      dma1((const GAS char*)p.k + uk, LB + A_K + 512 + w * 1024);
      dma1((const GAS char*)p.v + uv, LB + A_V + w * 1024);
      ALOAD("global_load_dwordx2", shw, (const GAS char*)p.shk + (unsigned)ch * 8);
      dma1((const GAS char*)p.q + uv, LB + A_Q + w * 1024);
      ALOAD("global_load_dwordx4", qs, (const GAS char*)p.q + (unsigned)(srow * D + 8 * lane) * 2);
      asm volatile("global_load_dword %0, %1, off sc1" : "=v"(tag) : "v"((const GAS char*)p.ws + OFF_EP) : "memory");
    }
    if (a == 0 && tid < 32) *(LAS v4u*)(L + A_K + 16 * tid) = v4u{0, 0, 0, 0};
    if (tid < 16) *(LAS unsigned*)(L + L_FLAG + 4 * tid) = 0u;
    if (tid < 128) *(LAS v4u*)(L + B_XC + (tid >> 5) * B_XCS + 4096 + 16 * (tid & 31)) = v4u{0, 0, 0, 0};
    asm volatile("s_waitcnt vmcnt(2)" : "+v"(shw) :: "memory");
    BARRIER();
    STAMPT(1);
    const int s0 = 64 * a + 8 * seg;
    const int r0 = 8 * seg + tq;
    const unsigned fr0 = (r0 >> 1) & 1, sg = seg & 1;
    const unsigned ua = ((w & 3) ^ (fr0 | (sg << 1))) * 32 + 8 * tp, ub = ((w & 3) ^ (fr0 | ((sg ^ 1) << 1))) * 32 + 8 * tp;
    {
      LAS unsigned char* kb = L + A_K + r0 * 128;
      const v2u k0 = trread(kb + ua), k1 = trread(kb + 512 + ua), k2 = trread(kb + 1024 + ub);
      LAS unsigned char* vb = L + A_V + r0 * 128 + ua;
      const v2u v0 = trread(vb), v1 = trread(vb + 512);
      LAS unsigned char* qb = L + A_Q + r0 * 128 + ua;
      const v2u q0 = trread(qb), q1 = trread(qb + 512);
      const unsigned E[6] = {k0.x, k0.y, k1.x, k1.y, k2.x, k2.y};
      unsigned O[5];
#pragma unroll
      for (int i = 0; i < 5; ++i) O[i] = __builtin_amdgcn_alignbit(E[i + 1], E[i], 16);
      const unsigned shx = shw.x, shy = shw.y;
      const unsigned w32 = __builtin_amdgcn_alignbit(shy, shy, 16);
      const unsigned w10 = __builtin_amdgcn_alignbit(shx, shx, 16);
      float ks[8];
#pragma unroll
      for (int j = 0; j < 8; ++j) {
        const unsigned pa = (j & 1) ? E[(j + 1) >> 1] : O[j >> 1];
        const unsigned pb = (j & 1) ? E[(j + 3) >> 1] : O[(j + 2) >> 1];
        ks[j] = dot2(pb, w10, dot2(pa, w32, 0.f));
      }
      const f32x2 a0 = f32x2{ks[0], ks[1]} * up2(v0.x), a1 = f32x2{ks[2], ks[3]} * up2(v0.y);
      const f32x2 a2 = f32x2{ks[4], ks[5]} * up2(v1.x), a3 = f32x2{ks[6], ks[7]} * up2(v1.y);
      const v4u xr = {pk(a3.y, a3.x), pk(a2.y, a2.x), pk(a1.y, a1.x), pk(a0.y, a0.x)};
      st16(xr, rs, OFF_XR + (ch * S + (S - 8 - s0)) * 2);
      st16(v4u{q0.x, q0.y, q1.x, q1.y}, rs, OFF_XT + (ch * S + s0) * 2);
    }
    asm volatile("s_waitcnt vmcnt(2)" : "+v"(qs), "+v"(tag) :: "memory");
    tag += 1u;
    {
      float s1 = dot2(qs.x, ONES, dot2(qs.y, ONES, 0.f)) + dot2(qs.z, ONES, dot2(qs.w, ONES, 0.f));
      float s2 = dot2(qs.x, qs.x, dot2(qs.y, qs.y, 0.f)) + dot2(qs.z, qs.z, dot2(qs.w, qs.w, 0.f));
      s1 = wavesum(s1); s2 = wavesum(s2);
      const float mean = s1 * (1.0f / D);
      float var = __builtin_fmaf(-mean, mean, s2 * (1.0f / D));
      var = var > 0.f ? var : 0.f;
      const float rstd = __builtin_amdgcn_rsqf(var + 1e-5f);
      if (lane == 63) __builtin_amdgcn_raw_buffer_store_b64(v2u{asu(-mean * rstd), asu(rstd)}, rs, OFF_ST + 8 * srow, 0, SC1);
    }
    STAMPT(2);
    if (w >= 4) {
      const int w4 = w - 4;
#pragma unroll
      for (int cc = 0; cc < 2; ++cc) {
        const int cw = 2 * w4 + cc;
        const u16* hrow = p.ssk + (size_t)(2 * cj + (cw >> 2)) * S;
        const int idx = 8 * (4 + lane) + 2 * (cw & 3) - 30;
        const unsigned lb = LB + B_HC + cw * B_HCS + 64;
        dma3((const GAS void*)(hrow + idx), lb);
        int idx2 = idx + 3 * 512;
        idx2 = idx2 > S - 8 ? S - 8 : idx2;
        dma1((const GAS void*)(hrow + idx2), lb + 3072);
      }
      const int wb = bid < 224 ? bid : 0;
#pragma unroll
      for (int cc = 0; cc < 2; ++cc) {
        const int wr = 8 * w4 + 4 * cc + g;
        dma1((const GAS char*)p.w1 + (unsigned)((32 * (wb / 14) + wr) * F + 128 * (wb % 14) + 8 * i16) * 2, LB + A_W1 + (2 * w4 + cc) * 1024);
        dma1((const GAS char*)p.w2 + (unsigned)((32 * (wb >> 2) + wr) * D + 128 * (wb & 3) + 8 * i16) * 2, LB + A_W2 + (2 * w4 + cc) * 1024);
      }
    }
    {
      const int hidx = (lane & 31) + 2 * q4 - 30;
      ALOAD("global_load_ushort", hval, (const GAS u16*)(p.ssk + (size_t)bch * S + (hidx < 0 ? 0 : hidx)));
      ALOAD("global_load_ushort", dv, (const GAS u16*)(p.ssd + bch));
    }
    if (w >= 4) { WAIT_V(14); } else { WAIT_V(2); }
    BARRIER();
    if (tid < 8) st4(tag, rs, OFF_FA1 + tid * 1024 + bid * 4);
    STAMPT(3);
    if (w == 0) poll4(rs, OFF_FA1 + xcd * 1024 + lane * 16, tag);
    BARRIER();
    STAMPT(4);
  }

  v4u wf1[16];
  v4u wf2[28];
  v4u b1a, b1b;
  const int kh = w >> 2, nq = w & 3;
  {
    const int xch = tid >> 8, xc = tid & 255;
    const unsigned xoff = OFF_XR + ((2 * cj + xch) * S + 8 * xc) * 2;
    const int c = i16, al = c & 1;
    const int tile = 2 * q4 + al;
    const int s0 = 256 * tile + 8 * g + 32 * (c >> 1);
    v4u x1, xt; unsigned nx;
    asm volatile("global_load_dwordx4 %0, %1, off sc1" : "=v"(x1) : "v"((const GAS char*)p.ws + xoff) : "memory");
    asm volatile("global_load_ushort %0, %1, off offset:16 sc1" : "=v"(nx) : "v"((const GAS char*)p.ws + xoff) : "memory");
    asm volatile("global_load_dwordx4 %0, %1, off sc1" : "=v"(xt) : "v"((const GAS char*)p.ws + OFF_XT + (bch * S + s0) * 2) : "memory");
    dma2s((const GAS char*)p.ws + OFF_ST + w * 2048 + lane * 16, LB + B_ST + w * 2048);
    asm volatile("s_waitcnt vmcnt(3)" : "+v"(x1), "+v"(nx), "+v"(hval), "+v"(dv) :: "memory");
    const float dch = bflo(dv);
    {
      if (lane < 32) *(LAS u16*)(L + B_HC + w * B_HCS + 2 * lane) = ((lane & 31) + 2 * q4 - 30) >= 0 ? (u16)hval : (u16)0;
      nx = xc < 255 ? nx : 0u;
      const v4u x0 = {__builtin_amdgcn_alignbit(x1.y, x1.x, 16), __builtin_amdgcn_alignbit(x1.z, x1.y, 16),
                      __builtin_amdgcn_alignbit(x1.w, x1.z, 16), __builtin_amdgcn_alignbit(nx, x1.w, 16)};
      LAS unsigned char* xb = L + B_XC + xch * 2 * B_XCS + 16 * xc;
      *(LAS v4u*)xb = x0;
      *(LAS v4u*)(xb + B_XCS) = x1;
    }
    BARRIER();
    STAMPT(5);
    if (w >= 4 && bid < 224) {
#pragma unroll
      for (int cc = 0; cc < 2; ++cc) {
        const int v = (w & 3) + 4 * cc;
        const int nb2 = 64 * (v & 3) + 32 * blk + 16 * (tp & 1) + 8 * (tp >> 1);
        const int row = 16 * cc + 8 * hh + tq;
        const v4u f1 = trfrag(L + A_W1 + row * 256 + nb2, 1024);
        const v4u f2 = trfrag(L + A_W2 + row * 256 + nb2, 1024);
        const int k16 = 2 * (bid / 14) + cc, nt = bid % 14;
        st16(f1, rs, OFF_W1 + (((nt * 8 + (k16 >> 4) * 4 + (v & 3)) * 16) + (k16 & 15)) * 1024 + lane * 16);
        const int ka = 2 * (bid >> 2) + cc, dt = 2 * (bid & 3) + ((v >> 1) & 1);
        st16(f2, rs, OFF_W2 + (((dt * 8 + ka / 14) * 28) + (ka % 14) * 2 + (v & 1)) * 1024 + lane * 16);
      }
    }
    SCHED();
    f32x4 acc[8];
#pragma unroll
    for (int i = 0; i < 8; ++i) acc[i] = f32x4{0.f, 0.f, 0.f, 0.f};
    LAS unsigned char* ab = L + B_HC + (chl * 4 + (c & 3)) * B_HCS + 2 * (224 + 8 * (c >> 2) + 8 * g) - 128 * q4;
    LAS unsigned char* bb = L + B_XC + (chl * 2 + al) * B_XCS + 2 * (2240 - 1792 - 32 * (c >> 1) + 8 * g) - 128 * q4;
    LAS f32x4* red = (LAS f32x4*)(L + B_RED);
    {
      v4u af[8], bf[8], ag[8], bg[8];
      bf[0] = *(LAS v4u*)(bb + 512 * 7);
#pragma unroll
      for (int i = 0; i < 8; ++i) af[i] = *(LAS v4u*)(ab + 512 * i);
#pragma unroll
      for (int i = 1; i < 8; ++i) bf[i] = *(LAS v4u*)(bb + 512 * (7 - i));
      SCHED();
#pragma unroll
      for (int ip = 0; ip < 8; ++ip) {
#pragma unroll
        for (int i = ip; i < 8; ++i) acc[i] = mfma16(af[i - ip], bf[ip], acc[i]);
        SCHED();
        ag[ip] = *(LAS v4u*)(ab - 64 + 512 * ip);
        bg[ip] = *(LAS v4u*)(bb - 64 + 512 * (7 - ip));
        SCHED();
      }
#pragma unroll
      for (int i = 0; i < 8; ++i) {
#pragma unroll
        for (int ip = 0; ip <= i; ++ip) acc[i] = mfma16(ag[i - ip], bg[ip], acc[i]);
        SCHED();
        if (i > 0) red[(w * 8 + i - 1) * 64 + lane] = acc[i - 1];
        SCHED();
      }
      red[(w * 8 + 7) * 64 + lane] = acc[7];
    }
    STAMPT(21);
    asm volatile("s_waitcnt vmcnt(0)" : "+v"(xt) :: "memory");
    BARRIER();
    if (tid < 8) st4(tag, rs, OFF_FA3 + tid * 1024 + bid * 4);
    STAMPT(6);
    f32x2 ya[2], yb[2];
    {
      LAS f32x4* rp = red + (chl * 32 + 2 * q4) * 64 + lane;
      const f32x4 a0 = rp[0], a1 = rp[8 * 64], a2 = rp[16 * 64], a3 = rp[24 * 64];
      const f32x4 b0 = rp[64], b1 = rp[9 * 64], b2 = rp[17 * 64], b3 = rp[25 * 64];
      ya[0] = (a0.xy + a1.xy) + (a2.xy + a3.xy); ya[1] = (a0.zw + a1.zw) + (a2.zw + a3.zw);
      yb[0] = (b0.xy + b1.xy) + (b2.xy + b3.xy); yb[1] = (b0.zw + b1.zw) + (b2.zw + b3.zw);
    }
    const v4u kvr = *(LAS v4u*)(L + B_XC + (chl * 2 + 1) * B_XCS + 2 * (S - 8 - s0));
    const unsigned kvp[4] = {kvr.w, kvr.z, kvr.y, kvr.x};
    const unsigned xp[4] = {xt.x, xt.y, xt.z, xt.w};
    unsigned ho[4];
#pragma unroll
    for (int r = 0; r < 4; ++r) {
      const f32x4 st = *(LAS f32x4*)(L + B_ST + 8 * s0 + 16 * r);
      const float va = ya[r >> 1][r & 1], vb = yb[r >> 1][r & 1];
      const float snd = al ? va : vb;
      const float rcv = asf(__builtin_amdgcn_update_dpp(0, asu(snd), 0xB1, 0xf, 0xf, true));
      const f32x2 y = {al ? rcv : va, al ? vb : rcv};
      const f32x2 xx = up2(xp[r]);
      const f32x2 kv = {bfhi(kvp[r]), bflo(kvp[r])};
      const f32x2 t = fma2(kv, f32x2{dch, dch}, y);
      const f32x2 qn = fma2(xx, st.yw, st.xz);
      const f32x2 h = fma2(qn, t, xx);
      ho[r] = pk(h.x, h.y);
    }
    {
      const int sl = (s0 & 63) >> 3;
      st16(v4u{ho[0], ho[1], ho[2], ho[3]}, rs, OFF_HT + ((s0 >> 6) * D + bch) * 128 + ((sl ^ (((bch >> 1) & 1) << 2)) << 4));
    }
    STAMPT(7);
    if (w == 0) poll4(rs, OFF_FA3 + xcd * 1024 + lane * 16, tag);
    WAIT_V(0);
    BARRIER();
    if (tid < 8) st4(tag, rs, OFF_FB + tid * 1024 + bid * 4);
    STAMPT(8);
  }

  {
    if (w == 0) poll4(rs, OFF_FB + xcd * 1024 + lane * 16, tag);
#pragma unroll
    for (int gi = 0; gi < 4; ++gi) {
      const unsigned vo = OFF_W1 + ((d_nt * 8 + w) * 16 + ((gi + nq) & 3) * 4) * 1024 + lane * 16;
      GLD(wf1[4 * gi], vo, p.ws, 0); GLD(wf1[4 * gi + 1], vo, p.ws, 1024); GLD(wf1[4 * gi + 2], vo, p.ws, 2048); GLD(wf1[4 * gi + 3], vo, p.ws, 3072);
    }
    asm volatile("global_load_dwordx4 %0, %1, %2" : "=v"(b1a) : "v"((unsigned)(128 * d_nt + 32 * nq + 8 * hh) * 2), "s"(p.b1) : "memory");
    asm volatile("global_load_dwordx4 %0, %1, %2 offset:32" : "=v"(b1b) : "v"((unsigned)(128 * d_nt + 32 * nq + 8 * hh) * 2), "s"(p.b1) : "memory");
    SCHED();
    BARRIER();
    STAMPT(9);
    if (d_act) {
      const GAS unsigned char* src = (const GAS unsigned char*)p.ws + OFF_HT + (size_t)(2 * d_mt) * 65536 + w * 8192 + lane * 16;
#pragma unroll
      for (int c = 0; c < 2; ++c)
#pragma unroll
        for (int i = 0; i < 2; ++i) dma4s(src + c * 65536 + i * 4096, LB + D_TILE + c * 65536 + w * 8192 + i * 4096);
    }
    {
      const int rr = 16 * (w & 3) + (lane >> 2);
      const int dch2 = 64 * e_dt + rr;
      dma1s((const GAS unsigned char*)p.ws + OFF_HT + ((2 * e_mt + (w >> 2)) * D + dch2) * 128 + (((unsigned)e_j ^ ((dch2 >> 1) & 1)) << 6) + 16 * (lane & 3),
            LB + E_RES + w * 1024);
    }
    SCHED();
    const unsigned w2off = OFF_W2 + (e_dt * 8 + w) * 28 * 1024 + lane * 16;
    if (d_act) {
      asm volatile("s_waitcnt vmcnt(17)" : "+v"(wf1[0]), "+v"(wf1[1]), "+v"(wf1[2]), "+v"(wf1[3]), "+v"(wf1[4]), "+v"(wf1[5]), "+v"(wf1[6]), "+v"(wf1[7]), "+v"(b1a) :: "memory");
      asm volatile("" : "+v"(wf1[8]), "+v"(wf1[9]), "+v"(wf1[10]), "+v"(wf1[11]), "+v"(wf1[12]), "+v"(wf1[13]), "+v"(wf1[14]), "+v"(wf1[15]), "+v"(b1b) :: "memory");
      {
        float c0 = 0.f, c1 = 0.f;
#pragma unroll
        for (int i = 0; i < 16; ++i) {
          c0 = dot2(wf1[i].x, ONES, c0); c1 = dot2(wf1[i].y, ONES, c1);
          c0 = dot2(wf1[i].z, ONES, c0); c1 = dot2(wf1[i].w, ONES, c1);
        }
        const float t = halfsum(c0 + c1);
        if (lane < 32) *(LAS float*)(L + D_CS + (w * 32 + lane) * 4) = t;
      }
      const unsigned basel = (unsigned)(D_TILE + (256 * kh + 8 * hh + tq) * 128 + 32 * blk + 8 * tp);
      const unsigned fsw = ((tq >> 1) & 1) << 6;
      const unsigned adr[2] = {basel + ((unsigned)(kh << 6) ^ fsw), basel + ((unsigned)((kh ^ 1) << 6) ^ fsw)};
      f32x16 acc[2][2];
      auto fraddr = [&](int c, int m) { return L + (adr[m & 1] + (unsigned)(c * 65536 + 8192 * ((((m >> 1) >> 2) + nq) & 3)) + 2048 * ((m >> 1) & 3)); };
      auto mstep = [&](int c) {
        float s1[2][2] = {{0.f, 0.f}, {0.f, 0.f}}, s2[2][2] = {{0.f, 0.f}, {0.f, 0.f}};
#pragma unroll
        for (int r = 0; r < 2; ++r)
#pragma unroll
          for (int e = 0; e < 16; ++e) acc[c][r][e] = 0.f;
        v4u fr[4];
#pragma unroll
        for (int n = 0; n < 3; ++n) fr[n] = trfrag(fraddr(c, n), 512);
        SCHED();
#pragma unroll
        for (int n = 0; n < 32; ++n) {
          const int i = n >> 1, r = n & 1;
          if (n + 3 < 32) fr[(n + 3) & 3] = trfrag(fraddr(c, n + 3), 512);
          acc[c][r] = mfma32(wf1[i], fr[n & 3], acc[c][r]);
          if (i < 4) {
            const v4u hf = fr[n & 3];
            s1[r][0] = dot2(hf.x, ONES, s1[r][0]); s2[r][0] = dot2(hf.x, hf.x, s2[r][0]);
            s1[r][1] = dot2(hf.y, ONES, s1[r][1]); s2[r][1] = dot2(hf.y, hf.y, s2[r][1]);
            s1[r][0] = dot2(hf.z, ONES, s1[r][0]); s2[r][0] = dot2(hf.z, hf.z, s2[r][0]);
            s1[r][1] = dot2(hf.w, ONES, s1[r][1]); s2[r][1] = dot2(hf.w, hf.w, s2[r][1]);
          }
          if (n >= 18) wf2[14 * c + n - 18] = ld16(rs, w2off + (14 * c + n - 18) * 1024);
          SCHED();
        }
#pragma unroll
        for (int r = 0; r < 2; ++r) {
          const float a = halfsum(s1[r][0] + s1[r][1]), b = halfsum(s2[r][0] + s2[r][1]);
          if (lane < 32) *(LAS f32x2*)(L + D_STAT + (((c * 8 + w) * 2 + ((r + kh) & 1)) * 32 + lane) * 8) = f32x2{a, b};
        }
      };
      auto xwrite = [&](int c) {
        LAS f32x4* ex = (LAS f32x4*)(L + D_TILE + c * 32768) + w * 256 + lane;
#pragma unroll
        for (int qd = 0; qd < 4; ++qd) ex[qd * 64] = f32x4{acc[c][1][4 * qd], acc[c][1][4 * qd + 1], acc[c][1][4 * qd + 2], acc[c][1][4 * qd + 3]};
      };
      auto estep = [&](int c) {
        LAS f32x4* ex = (LAS f32x4*)(L + D_TILE + c * 32768) + (w ^ 4) * 256 + lane;
        f32x2 sa = {0.f, 0.f};
#pragma unroll
        for (int ww = 0; ww < 8; ++ww) sa += *(LAS f32x2*)(L + D_STAT + (((c * 8 + ww) * 2 + kh) * 32 + (lane & 31)) * 8);
        const float mean = sa.x * (1.0f / D);
        float var = __builtin_fmaf(-mean, mean, sa.y * (1.0f / D));
        var = var > 0.f ? var : 0.f;
        const float r1 = __builtin_amdgcn_rsqf(var + 1e-5f);
        const float rstd = r1 * __builtin_amdgcn_rsqf(__builtin_fmaf(var * r1, r1, 1e-5f));
        const float nm = -rstd * mean;
        const f32x2 rs2 = {rstd, rstd}, nm2 = {nm, nm};
        unsigned o[8];
        LAS f32x4* c4 = (LAS f32x4*)(L + D_CS) + nq * 8 + hh;
        const unsigned bb[8] = {b1a.x, b1a.y, b1a.z, b1a.w, b1b.x, b1b.y, b1b.z, b1b.w};
#pragma unroll
        for (int qd = 0; qd < 4; ++qd) {
          const f32x4 pp = ex[qd * 64];
          const f32x4 cs = c4[2 * qd] + c4[32 + 2 * qd];
#pragma unroll
          for (int hf = 0; hf < 2; ++hf) {
            const int e = 2 * qd + hf;
            const f32x2 a = f32x2{acc[c][0][2 * e], acc[c][0][2 * e + 1]} + (hf ? pp.zw : pp.xy);
            const f32x2 x = fma2(rs2, a, fma2(nm2, hf ? cs.zw : cs.xy, up2(bb[e])));
            const f32x2 pz = fma2(x * x, f32x2{-0.10294324f, -0.10294324f}, f32x2{-2.3022082f, -2.3022082f});
            const f32x2 z = x * pz;
            const f32x2 den = f32x2{__builtin_amdgcn_exp2f(z.x), __builtin_amdgcn_exp2f(z.y)} + f32x2{1.f, 1.f};
            const f32x2 y = x * f32x2{__builtin_amdgcn_rcpf(den.x), __builtin_amdgcn_rcpf(den.y)};
            o[e] = pk(y.x, y.y);
          }
        }
        const unsigned ho = OFF_HF + ((4 * d_mt + 2 * c + kh) * 112 + 8 * d_nt + 2 * nq) * 1024 + lane * 16;
        st16(v4u{o[0], o[1], o[2], o[3]}, rs, ho);
        st16(v4u{o[4], o[5], o[6], o[7]}, rs, ho + 1024);
      };
      WAIT_V(9);
      BARRIER();
      STAMPT(10);
      SCHED();
      mstep(0);
      STAMPT(22);
      WAIT_V(14);
      BARRIER();
      STAMPT(11);
      xwrite(0);
      BARRIER();
      STAMPT(23);
      estep(0);
      STAMPT(24);
      mstep(1);
      STAMPT(25);
      xwrite(1);
      WAIT_V(14);
      BARRIER();
      if (tid == 0) st4(tag, rs, OFF_FD + d_mt * 128 + d_nt * 4);
      STAMPT(12);
      estep(1);
      STAMPT(13);
    } else {
#pragma unroll
      for (int i = 0; i < 28; ++i) wf2[i] = ld16(rs, w2off + i * 1024);
    }
  }

  {
    const int sbo = w >> 2, dbo = (w >> 1) & 1, hf = w & 1;
    const v4u b2v = *(const GAS v4u*)((const GAS char*)p.b2 + (unsigned)(64 * e_dt + 32 * dbo + 16 * hf + 8 * hh) * 2);
    if (w == 0) poll1(rs, OFF_FD + e_mt * 128 + (lane < 14 ? lane : 0) * 4, tag);
    WAIT_V(0);
    BARRIER();
    if (d_act && tid == 0) st4(tag, rs, OFF_FD + 2048 + d_mt * 128 + d_nt * 4);
    STAMPT(14);
    const unsigned hoff0 = OFF_HF + ((4 * e_mt + e_j) * 112 + 14 * w) * 1024 + lane * 16, hoff1 = hoff0 + 2 * 112 * 1024;
    if (w == 0) { poll1(rs, OFF_FD + 2048 + e_mt * 128 + (lane < 14 ? lane : 0) * 4, tag); lds_set(L, 1); }
    v4u hf0[14], hf1[14];
#pragma unroll
    for (int k = 0; k < 14; ++k) hf0[k] = ld16(rs, hoff0 + k * 1024);
    SCHED();
    lds_wait(L, 1);
    STAMPT(15);
    LAS f32x4* red = (LAS f32x4*)(L + E_RED + w * 16384) + lane;
    {
      f32x16 acc[2];
#pragma unroll
      for (int e = 0; e < 16; ++e) { acc[0][e] = 0.f; acc[1][e] = 0.f; }
#pragma unroll
      for (int k = 0; k < 14; ++k) {
        acc[0] = mfma32(wf2[2 * k], hf0[k], acc[0]); acc[1] = mfma32(wf2[2 * k + 1], hf0[k], acc[1]);
        SCHED();
        hf1[k] = ld16(rs, hoff1 + k * 1024);
        SCHED();
      }
#pragma unroll
      for (int d = 0; d < 2; ++d)
#pragma unroll
        for (int qd = 0; qd < 4; ++qd) red[(d * 4 + qd) * 64] = f32x4{acc[d][4 * qd], acc[d][4 * qd + 1], acc[d][4 * qd + 2], acc[d][4 * qd + 3]};
    }
    STAMPT(16);
    {
      f32x16 acc[2];
#pragma unroll
      for (int e = 0; e < 16; ++e) { acc[0][e] = 0.f; acc[1][e] = 0.f; }
#pragma unroll
      for (int k = 0; k < 14; ++k) { acc[0] = mfma32(wf2[2 * k], hf1[k], acc[0]); acc[1] = mfma32(wf2[2 * k + 1], hf1[k], acc[1]); }
#pragma unroll
      for (int d = 0; d < 2; ++d)
#pragma unroll
        for (int qd = 0; qd < 4; ++qd) red[512 + (d * 4 + qd) * 64] = f32x4{acc[d][4 * qd], acc[d][4 * qd + 1], acc[d][4 * qd + 2], acc[d][4 * qd + 3]};
    }
    BARRIER();
    STAMPT(17);
    f32x2 o[4] = {{0.f, 0.f}, {0.f, 0.f}, {0.f, 0.f}, {0.f, 0.f}};
#pragma unroll
    for (int ww = 0; ww < 8; ++ww) {
      LAS f32x4* rp = (LAS f32x4*)(L + E_RED + ww * 16384) + sbo * 512 + (dbo * 4 + 2 * hf) * 64 + lane;
      const f32x4 a = rp[0], b = rp[64];
      o[0] += a.xy; o[1] += a.zw; o[2] += b.xy; o[3] += b.zw;
    }
    const v4u hr = trfrag(L + E_RES + sbo * 4096 + (32 * dbo + 16 * hf + 8 * hh + tq) * 64 + 32 * blk + 8 * tp, 256);
    o[0] += up2(b2v.x) + up2(hr.x); o[1] += up2(b2v.y) + up2(hr.y);
    o[2] += up2(b2v.z) + up2(hr.z); o[3] += up2(b2v.w) + up2(hr.w);
    const v4u ov = {pk(o[0].x, o[0].y), pk(o[1].x, o[1].y), pk(o[2].x, o[2].y), pk(o[3].x, o[3].y)};
    *(GAS v4u*)((GAS char*)p.out + (unsigned)((128 * e_mt + 64 * sbo + 32 * e_j + (lane & 31)) * D + 64 * e_dt + 32 * dbo + 16 * hf + 8 * hh) * 2) = ov;
    if (bid == 0 && tid == 0) st4(tag, rs, OFF_EP);
    STAMPT(18);
  }
}

extern "C" void kernel_launch(void* const* d_in, const int* in_sizes, int n_in, void* d_out, int out_size, void* d_ws,
                              size_t ws_size, hipStream_t stream) {
  Params p;
  p.q = (const u16*)d_in[0]; p.k = (const u16*)d_in[1]; p.v = (const u16*)d_in[2];
  p.shk = (const u16*)d_in[3]; p.ssk = (const u16*)d_in[4]; p.ssd = (const u16*)d_in[5];
  p.w1 = (const u16*)d_in[7]; p.b1 = (const u16*)d_in[8]; p.w2 = (const u16*)d_in[9]; p.b2 = (const u16*)d_in[10];
  p.out = (u16*)d_out; p.ws = (unsigned char*)d_ws;
  fused<<<256, 512, 0, stream>>>(p);
}
```

```cpp
#include <hip/hip_runtime.h>
#include <hip/hip_bf16.h>

#define GAS __attribute__((address_space(1)))
#define LAS __attribute__((address_space(3)))
typedef unsigned short u16;
typedef unsigned v4u __attribute__((ext_vector_type(4)));
typedef unsigned v2u __attribute__((ext_vector_type(2)));
typedef float f32x16 __attribute__((ext_vector_type(16)));
typedef float f32x4 __attribute__((ext_vector_type(4)));
typedef float f32x2 __attribute__((ext_vector_type(2)));
typedef short s16x4 __attribute__((ext_vector_type(4)));
typedef short bf16x8 __attribute__((ext_vector_type(8)));
typedef __bf16 bf2 __attribute__((ext_vector_type(2)));
typedef __amdgpu_buffer_rsrc_t rsrc_t;

constexpr int S = 2048, D = 512, F = 1792;
constexpr unsigned OFF_EP = 0;
constexpr unsigned OFF_FA1 = 4096;
constexpr unsigned OFF_FD = 8192;
constexpr unsigned OFF_FA2 = 16384;
constexpr unsigned OFF_FA3 = OFF_FA2 + 32768;
constexpr unsigned OFF_FB = OFF_FA3 + 32768;
constexpr unsigned OFF_XR = 131072;
constexpr unsigned OFF_XT = OFF_XR + D * S * 2;
constexpr unsigned OFF_HT = OFF_XT + D * S * 2;
constexpr unsigned OFF_ST = OFF_HT + D * S * 2;
constexpr unsigned OFF_W1 = OFF_ST + 16384;
constexpr unsigned OFF_W2 = OFF_W1 + D * F * 2;
constexpr unsigned OFF_HF = OFF_W2 + D * F * 2;
constexpr unsigned WS_END = OFF_HF + S * F * 2;
constexpr int SPIN_CAP = 1 << 16;

struct Params {
  const u16 *q, *k, *v, *shk, *ssk, *ssd, *w1, *b1, *w2, *b2;
  u16* out;
  unsigned char* ws;
};

__device__ __forceinline__ float asf(unsigned u) { return __builtin_bit_cast(float, u); }
__device__ __forceinline__ unsigned asu(float f) { return __builtin_bit_cast(unsigned, f); }
__device__ __forceinline__ float bflo(unsigned u) { return asf(u << 16); }
__device__ __forceinline__ float bfhi(unsigned u) { return asf(u & 0xffff0000u); }
__device__ __forceinline__ unsigned pk(float lo, float hi) {
  f32x2 f = {lo, hi};
  return __builtin_bit_cast(unsigned, __builtin_convertvector(f, bf2));
}
__device__ __forceinline__ f32x2 up2(unsigned u) { return f32x2{bflo(u), bfhi(u)}; }
__device__ __forceinline__ f32x2 fma2(f32x2 a, f32x2 b, f32x2 c) { return __builtin_elementwise_fma(a, b, c); }
__device__ __forceinline__ f32x4 mfma16(v4u a, v4u b, f32x4 c) {
  return __builtin_amdgcn_mfma_f32_16x16x32_bf16(__builtin_bit_cast(bf16x8, a), __builtin_bit_cast(bf16x8, b), c, 0, 0, 0);
}
__device__ __forceinline__ f32x16 mfma32(v4u a, v4u b, f32x16 c) {
  return __builtin_amdgcn_mfma_f32_32x32x16_bf16(__builtin_bit_cast(bf16x8, a), __builtin_bit_cast(bf16x8, b), c, 0, 0, 0);
}
__device__ __forceinline__ v2u trread(LAS unsigned char* p) {
  return __builtin_bit_cast(v2u, __builtin_amdgcn_ds_read_tr16_b64_v4i16((LAS s16x4*)p));
}
__device__ __forceinline__ v4u trfrag(LAS unsigned char* p, int second) {
  const v2u a = trread(p), b = trread(p + second);
  return v4u{a.x, a.y, b.x, b.y};
}
__device__ __forceinline__ float dot2(unsigned a, unsigned b, float c) {
  return __builtin_amdgcn_fdot2_f32_bf16(__builtin_bit_cast(bf2, a), __builtin_bit_cast(bf2, b), c, false);
}
constexpr int SC1 = 16;
#define DMA_HEAD "s_mov_b32 m0, %1\n\ts_nop 0\n\t"
#define DMA_P(o, f) "global_load_lds_dwordx4 %0, off offset:" #o f "\n\t"
__device__ __forceinline__ void dma4(const GAS void* g, unsigned lds) {
  asm volatile(DMA_HEAD DMA_P(0, "") DMA_P(1024, "") DMA_P(2048, "") DMA_P(3072, "") :: "v"(g), "s"(lds) : "memory");
}
__device__ __forceinline__ void dma3(const GAS void* g, unsigned lds) {
  asm volatile(DMA_HEAD DMA_P(0, "") DMA_P(1024, "") DMA_P(2048, "") :: "v"(g), "s"(lds) : "memory");
}
__device__ __forceinline__ void dma1(const GAS void* g, unsigned lds) {
  asm volatile(DMA_HEAD DMA_P(0, "") :: "v"(g), "s"(lds) : "memory");
}
__device__ __forceinline__ void dma4s(const GAS void* g, unsigned lds) {
  asm volatile(DMA_HEAD DMA_P(0, " sc1") DMA_P(1024, " sc1") DMA_P(2048, " sc1") DMA_P(3072, " sc1") :: "v"(g), "s"(lds) : "memory");
}
__device__ __forceinline__ void dma3s(const GAS void* g, unsigned lds) {
  asm volatile(DMA_HEAD DMA_P(0, " sc1") DMA_P(1024, " sc1") DMA_P(2048, " sc1") :: "v"(g), "s"(lds) : "memory");
}
__device__ __forceinline__ void dma2s(const GAS void* g, unsigned lds) {
  asm volatile(DMA_HEAD DMA_P(0, " sc1") DMA_P(1024, " sc1") :: "v"(g), "s"(lds) : "memory");
}
__device__ __forceinline__ void dma1s(const GAS void* g, unsigned lds) {
  asm volatile(DMA_HEAD DMA_P(0, " sc1") :: "v"(g), "s"(lds) : "memory");
}
__device__ __forceinline__ v4u ld16(rsrc_t rs, unsigned off) { return __builtin_amdgcn_raw_buffer_load_b128(rs, off, 0, SC1); }
__device__ __forceinline__ void st16(v4u v, rsrc_t rs, unsigned off) { __builtin_amdgcn_raw_buffer_store_b128(v, rs, off, 0, SC1); }
__device__ __forceinline__ void st4(unsigned v, rsrc_t rs, unsigned off) { __builtin_amdgcn_raw_buffer_store_b32(v, rs, off, 0, SC1); }
template <int CTRL, int RM>
__device__ __forceinline__ float dppadd(float x) {
  return x + asf(__builtin_amdgcn_update_dpp(0, asu(x), CTRL, RM, 0xf, true));
}
__device__ __forceinline__ float wavesum(float x) {
  x = dppadd<0x111, 0xf>(x); x = dppadd<0x112, 0xf>(x); x = dppadd<0x114, 0xf>(x); x = dppadd<0x118, 0xf>(x);
  x = dppadd<0x142, 0xa>(x); x = dppadd<0x143, 0xc>(x);
  return x;
}
__device__ __forceinline__ float halfsum(float x) {
  const unsigned u = asu(x);
  auto r = __builtin_amdgcn_permlane32_swap(u, u, false, false);
  const unsigned r0 = r[0], r1 = r[1];
  return asf(r0) + asf(r1);
}
#define WAIT_V(n) asm volatile("s_waitcnt vmcnt(" #n ")" ::: "memory")
#define BARRIER() do { asm volatile("s_waitcnt lgkmcnt(0)" ::: "memory"); __builtin_amdgcn_s_barrier(); asm volatile("" ::: "memory"); } while (0)
#define SCHED() __builtin_amdgcn_sched_barrier(0)
constexpr unsigned ONES = 0x3f803f80u;

#define STAMPT(idx)

#define TOUCH 0
constexpr unsigned CODE_BYTES = 18432;
constexpr int LDS_BYTES = 163840;
constexpr int A_K = 0, A_V = A_K + 68 * 128, A_Q = A_V + 8192, A_END = A_Q + 8192;
constexpr int B_HCS = 4160, B_XCS = 4640;
constexpr int L_FLAG = LDS_BYTES - 64;
constexpr int B_XC = L_FLAG - 4 * B_XCS, B_HC = B_XC - 8 * B_HCS, B_RED = 0, B_ST = 65536, A_W1 = B_ST + 16384, A_W2 = A_W1 + 8192;
constexpr int D_TILE = 0, D_STAT = 131072, D_CS = D_STAT + 2 * 8 * 64 * 8, D_END = D_CS + 1024;
constexpr int E_RED = 0, E_RES = 143360;
static_assert(A_W2 + 8192 <= B_HC && A_END <= 65536 && B_HC % 16 == 0 && D_END <= E_RES && E_RES + 8192 <= L_FLAG, "");

__device__ __forceinline__ void poll4(rsrc_t rs, unsigned off, unsigned tag) {
  for (int spins = 0; spins < SPIN_CAP; ++spins) {
    const unsigned f0 = __builtin_amdgcn_raw_buffer_load_b32(rs, off, 0, SC1), f1 = __builtin_amdgcn_raw_buffer_load_b32(rs, off + 1024, 0, SC1);
    const unsigned f2 = __builtin_amdgcn_raw_buffer_load_b32(rs, off + 2048, 0, SC1), f3 = __builtin_amdgcn_raw_buffer_load_b32(rs, off + 3072, 0, SC1);
    if (__builtin_amdgcn_ballot_w64(f0 == tag && f1 == tag && f2 == tag && f3 == tag) == ~0ull) break;
    __builtin_amdgcn_s_sleep(1);
    asm volatile("" ::: "memory");
  }
}
__device__ __forceinline__ void poll1(rsrc_t rs, unsigned off, unsigned tag) {
  for (int spins = 0; spins < SPIN_CAP; ++spins) {
    const unsigned f = __builtin_amdgcn_raw_buffer_load_b32(rs, off, 0, SC1);
    if (__builtin_amdgcn_ballot_w64(f == tag) == ~0ull) break;
    __builtin_amdgcn_s_sleep(1);
    asm volatile("" ::: "memory");
  }
}
__device__ __forceinline__ void lds_set(LAS unsigned char* L, int idx) {
  asm volatile("" ::: "memory");
  *(volatile LAS unsigned*)(L + L_FLAG + 4 * idx) = 1u;
}
__device__ __forceinline__ void lds_wait(LAS unsigned char* L, int idx) {
  for (int spins = 0; spins < (SPIN_CAP << 4); ++spins) {
    if (__builtin_amdgcn_readfirstlane(*(volatile LAS unsigned*)(L + L_FLAG + 4 * idx)) != 0) break;
    __builtin_amdgcn_s_sleep(1);
  }
  asm volatile("" ::: "memory");
}
#define GLD(dst, voff, base, imm) asm volatile("global_load_dwordx4 %0, %1, %2 offset:" #imm " sc1" : "=v"(dst) : "v"(voff), "s"(base) : "memory")
__device__ __forceinline__ void lds_count_flag(LAS unsigned char* L, int idx, unsigned n, int tid, bool act, unsigned tag, rsrc_t rs, unsigned off) {
  if ((tid & 63) == 0) {
    const unsigned old = __hip_atomic_fetch_add((LAS unsigned*)(L + L_FLAG + 4 * idx), 1u, __ATOMIC_RELAXED, __HIP_MEMORY_SCOPE_WORKGROUP);
    if (old == n - 1 && act) st4(tag, rs, off);
  }
}
__device__ __forceinline__ void lds_count_flag8(LAS unsigned char* L, int idx, int lane, unsigned tag, rsrc_t rs, unsigned off) {
  unsigned old = 0;
  if (lane == 0) old = __hip_atomic_fetch_add((LAS unsigned*)(L + L_FLAG + 4 * idx), 1u, __ATOMIC_RELAXED, __HIP_MEMORY_SCOPE_WORKGROUP);
  if (__builtin_amdgcn_readfirstlane(old) == 7 && lane < 8) st4(tag, rs, off + lane * 4096);
}
#define ALOAD(op, dst, ptr) asm volatile(op " %0, %1, off" : "=v"(dst) : "v"(ptr) : "memory")

__global__ void __launch_bounds__(512, 2) fused(Params p) {
  __shared__ __attribute__((aligned(16))) unsigned char smem[LDS_BYTES];
  LAS unsigned char* L = (LAS unsigned char*)smem;
  const unsigned LB = (unsigned)(size_t)L;
  const int tid = threadIdx.x, lane = tid & 63, w = __builtin_amdgcn_readfirstlane(tid >> 6);
  const int bid = blockIdx.x, xcd = bid & 7, bix = bid >> 3;
  const rsrc_t rs = __builtin_amdgcn_make_buffer_rsrc((void*)p.ws, 0, (int)WS_END, 0x00020000);
  const int g = lane >> 4, i16 = lane & 15, tq = i16 >> 2, tp = i16 & 3;
  const int hh = lane >> 5, blk = g & 1;
  STAMPT(0);

  const int cj = xcd * 32 + bix;
  const bool d_act = bix < 28;
  const int d_mt = 4 * (xcd >> 1) + (d_act ? bix / 7 : 0), d_nt = 7 * (xcd & 1) + bix % 7;
  const int e_mt = 2 * xcd + (bix >> 4), e_j = (bix >> 3) & 1, e_dt = bix & 7;
  const int chl = w >> 2, q4 = w & 3, bch = 2 * cj + chl;

  unsigned tag;
  unsigned hval, dv;
  {
    const int b = xcd, a = bix;
    const int ch = 64 * b + 16 * (w & 3) + i16, seg = 4 * (w >> 2) + g;
    const int srow = 64 * a + 8 * b + w;
    v2u shw; v4u qs;
    unsigned dm1 = 0, dm2 = 0;
    {
      const int rl = lane >> 3, sl = lane & 7;
      const int rhok = 4 + 8 * w + rl, rhov = 8 * w + rl;
      const unsigned fk = ((rhok >> 1) & 1) | (((rhok >> 3) & 1) << 1), fv = ((rhov >> 1) & 1) | (((rhov >> 3) & 1) << 1);
      const unsigned rowoff = ((64 * a + 8 * w + rl) * D + 64 * b) * 2 + 16 * (sl & 1);
      const unsigned uk = rowoff + 32 * ((sl >> 1) ^ fk), uv = rowoff + 32 * ((sl >> 1) ^ fv);
      if (w == 0 && a > 0 && lane < 32)
        dma1((const GAS char*)p.k + (((64 * a - 4 + rl) * D + 64 * b) * 2 + 16 * (sl & 1) + 32 * ((sl >> 1) ^ ((rl >> 1) & 1))), LB + A_K);
      ALOAD("global_load_dwordx2", shw, (const GAS char*)p.shk + (unsigned)ch * 8);
      dma1((const GAS char*)p.k + uk, LB + A_K + 512 + w * 1024);
      dma1((const GAS char*)p.v + uv, LB + A_V + w * 1024);
      dma1((const GAS char*)p.q + uv, LB + A_Q + w * 1024);
      ALOAD("global_load_dwordx4", qs, (const GAS char*)p.q + (unsigned)(srow * D + 8 * lane) * 2);
      asm volatile("global_load_dword %0, %1, off sc1" : "=v"(tag) : "v"((const GAS char*)p.ws + OFF_EP) : "memory");
    }
    if (a == 0 && tid < 32) *(LAS v4u*)(L + A_K + 16 * tid) = v4u{0, 0, 0, 0};
    if (tid < 16) *(LAS unsigned*)(L + L_FLAG + 4 * tid) = 0u;
    if (tid < 128) *(LAS v4u*)(L + B_XC + (tid >> 5) * B_XCS + 4096 + 16 * (tid & 31)) = v4u{0, 0, 0, 0};
    asm volatile("s_waitcnt vmcnt(3)" : "+v"(shw) :: "memory");
    BARRIER();
    STAMPT(1);
    const int s0 = 64 * a + 8 * seg;
    const int r0 = 8 * seg + tq;
    const unsigned fr0 = (r0 >> 1) & 1, sg = seg & 1;
    const unsigned ua = ((w & 3) ^ (fr0 | (sg << 1))) * 32 + 8 * tp, ub = ((w & 3) ^ (fr0 | ((sg ^ 1) << 1))) * 32 + 8 * tp;
    {
      LAS unsigned char* kb = L + A_K + r0 * 128;
      const v2u k0 = trread(kb + ua), k1 = trread(kb + 512 + ua), k2 = trread(kb + 1024 + ub);
      LAS unsigned char* vb = L + A_V + r0 * 128 + ua;
      const v2u v0 = trread(vb), v1 = trread(vb + 512);
      const unsigned E[6] = {k0.x, k0.y, k1.x, k1.y, k2.x, k2.y};
      unsigned O[5];
#pragma unroll
      for (int i = 0; i < 5; ++i) O[i] = __builtin_amdgcn_alignbit(E[i + 1], E[i], 16);
      const unsigned shx = shw.x, shy = shw.y;
      const unsigned w32 = __builtin_amdgcn_alignbit(shy, shy, 16);
      const unsigned w10 = __builtin_amdgcn_alignbit(shx, shx, 16);
      float ks[8];
#pragma unroll
      for (int j = 0; j < 8; ++j) {
        const unsigned pa = (j & 1) ? E[(j + 1) >> 1] : O[j >> 1];
        const unsigned pb = (j & 1) ? E[(j + 3) >> 1] : O[(j + 2) >> 1];
        ks[j] = dot2(pb, w10, dot2(pa, w32, 0.f));
      }
      const f32x2 a0 = f32x2{ks[0], ks[1]} * up2(v0.x), a1 = f32x2{ks[2], ks[3]} * up2(v0.y);
      const f32x2 a2 = f32x2{ks[4], ks[5]} * up2(v1.x), a3 = f32x2{ks[6], ks[7]} * up2(v1.y);
      const v4u xr = {pk(a3.y, a3.x), pk(a2.y, a2.x), pk(a1.y, a1.x), pk(a0.y, a0.x)};
      st16(xr, rs, OFF_XR + (ch * S + (S - 8 - s0)) * 2);
    }
    asm volatile("s_waitcnt vmcnt(1)" : "+v"(qs), "+v"(tag) :: "memory");
    tag += 1u;
    {
      float s1 = dot2(qs.x, ONES, dot2(qs.y, ONES, 0.f)) + dot2(qs.z, ONES, dot2(qs.w, ONES, 0.f));
      float s2 = dot2(qs.x, qs.x, dot2(qs.y, qs.y, 0.f)) + dot2(qs.z, qs.z, dot2(qs.w, qs.w, 0.f));
      s1 = wavesum(s1); s2 = wavesum(s2);
      const float mean = s1 * (1.0f / D);
      float var = __builtin_fmaf(-mean, mean, s2 * (1.0f / D));
      var = var > 0.f ? var : 0.f;
      const float rstd = __builtin_amdgcn_rsqf(var + 1e-5f);
      if (lane == 63) __builtin_amdgcn_raw_buffer_store_b64(v2u{asu(-mean * rstd), asu(rstd)}, rs, OFF_ST + 8 * srow, 0, SC1);
    }
    STAMPT(2);
    asm volatile("s_waitcnt vmcnt(1)" : "+v"(dm1), "+v"(dm2) :: "memory");
    BARRIER();
    if (tid == 0) st4(tag, rs, OFF_FA1 + (b * 32 + a) * 16);
    STAMPT(3);
    {
      LAS unsigned char* qb = L + A_Q + r0 * 128 + ua;
      const v2u q0 = trread(qb), q1 = trread(qb + 512);
      st16(v4u{q0.x, q0.y, q1.x, q1.y}, rs, OFF_XT + (ch * S + s0) * 2);
    }
    SCHED();
    if (w >= 4) {
      const int w4 = w - 4;
#pragma unroll
      for (int cc = 0; cc < 2; ++cc) {
        const int cw = 2 * w4 + cc;
        const u16* hrow = p.ssk + (size_t)(2 * cj + (cw >> 2)) * S;
        const int idx = 8 * (4 + lane) + 2 * (cw & 3) - 30;
        const unsigned lb = LB + B_HC + cw * B_HCS + 64;
        dma3((const GAS void*)(hrow + idx), lb);
        int idx2 = idx + 3 * 512;
        idx2 = idx2 > S - 8 ? S - 8 : idx2;
        dma1((const GAS void*)(hrow + idx2), lb + 3072);
      }
      const int wb = bid < 224 ? bid : 0;
#pragma unroll
      for (int cc = 0; cc < 2; ++cc) {
        const int wr = 8 * w4 + 4 * cc + g;
        dma1((const GAS char*)p.w1 + (unsigned)((32 * (wb / 14) + wr) * F + 128 * (wb % 14) + 8 * i16) * 2, LB + A_W1 + (2 * w4 + cc) * 1024);
        dma1((const GAS char*)p.w2 + (unsigned)((32 * (wb >> 2) + wr) * D + 128 * (wb & 3) + 8 * i16) * 2, LB + A_W2 + (2 * w4 + cc) * 1024);
      }
    }
    {
      const int hidx = (lane & 31) + 2 * q4 - 30;
      ALOAD("global_load_ushort", hval, (const GAS u16*)(p.ssk + (size_t)bch * S + (hidx < 0 ? 0 : hidx)));
      ALOAD("global_load_ushort", dv, (const GAS u16*)(p.ssd + bch));
    }
    if (w >= 4) { WAIT_V(14); } else { WAIT_V(2); }
    lds_count_flag8(L, 3, lane, tag, rs, OFF_FA2 + bid * 16);
    if (w == 0) poll1(rs, OFF_FA1 + (xcd * 32 + (lane & 31)) * 16, tag);
    BARRIER();
    STAMPT(4);
  }

  v4u wf1[16];
  v4u wf2[28];
  v4u b1a, b1b;
  const int kh = w >> 2, nq = w & 3;
  {
    const int xch = tid >> 8, xc = tid & 255;
    const unsigned xoff = OFF_XR + ((2 * cj + xch) * S + 8 * xc) * 2;
    const int c = i16, al = c & 1;
    const int tile = 2 * q4 + al;
    const int s0 = 256 * tile + 8 * g + 32 * (c >> 1);
    v4u x1, xt; unsigned nx;
    asm volatile("global_load_dwordx4 %0, %1, off sc1" : "=v"(x1) : "v"((const GAS char*)p.ws + xoff) : "memory");
    asm volatile("global_load_ushort %0, %1, off offset:16 sc1" : "=v"(nx) : "v"((const GAS char*)p.ws + xoff) : "memory");
    asm volatile("s_waitcnt vmcnt(0)" : "+v"(x1), "+v"(nx), "+v"(hval), "+v"(dv) :: "memory");
    const float dch = bflo(dv);
    {
      if (lane < 32) *(LAS u16*)(L + B_HC + w * B_HCS + 2 * lane) = ((lane & 31) + 2 * q4 - 30) >= 0 ? (u16)hval : (u16)0;
      nx = xc < 255 ? nx : 0u;
      const v4u x0 = {__builtin_amdgcn_alignbit(x1.y, x1.x, 16), __builtin_amdgcn_alignbit(x1.z, x1.y, 16),
                      __builtin_amdgcn_alignbit(x1.w, x1.z, 16), __builtin_amdgcn_alignbit(nx, x1.w, 16)};
      LAS unsigned char* xb = L + B_XC + xch * 2 * B_XCS + 16 * xc;
      *(LAS v4u*)xb = x0;
      *(LAS v4u*)(xb + B_XCS) = x1;
    }
    BARRIER();
    STAMPT(5);
    if (w == 0) { poll4(rs, OFF_FA2 + xcd * 4096 + lane * 16, tag); lds_set(L, 0); }
    if (w >= 4 && bid < 224) {
#pragma unroll
      for (int cc = 0; cc < 2; ++cc) {
        const int v = (w & 3) + 4 * cc;
        const int nb2 = 64 * (v & 3) + 32 * blk + 16 * (tp & 1) + 8 * (tp >> 1);
        const int row = 16 * cc + 8 * hh + tq;
        const v4u f1 = trfrag(L + A_W1 + row * 256 + nb2, 1024);
        const v4u f2 = trfrag(L + A_W2 + row * 256 + nb2, 1024);
        const int k16 = 2 * (bid / 14) + cc, nt = bid % 14;
        st16(f1, rs, OFF_W1 + (((nt * 8 + (k16 >> 4) * 4 + (v & 3)) * 16) + (k16 & 15)) * 1024 + lane * 16);
        const int ka = 2 * (bid >> 2) + cc, dt = 2 * (bid & 3) + ((v >> 1) & 1);
        st16(f2, rs, OFF_W2 + (((dt * 8 + ka / 14) * 28) + (ka % 14) * 2 + (v & 1)) * 1024 + lane * 16);
      }
    }
    SCHED();
    f32x4 acc[8];
#pragma unroll
    for (int i = 0; i < 8; ++i) acc[i] = f32x4{0.f, 0.f, 0.f, 0.f};
    LAS unsigned char* ab = L + B_HC + (chl * 4 + (c & 3)) * B_HCS + 2 * (224 + 8 * (c >> 2) + 8 * g) - 128 * q4;
    LAS unsigned char* bb = L + B_XC + (chl * 2 + al) * B_XCS + 2 * (2240 - 1792 - 32 * (c >> 1) + 8 * g) - 128 * q4;
    LAS f32x4* red = (LAS f32x4*)(L + B_RED);
    {
      v4u af[8], bf[8], ag[8], bg[8];
      bf[0] = *(LAS v4u*)(bb + 512 * 7);
#pragma unroll
      for (int i = 0; i < 8; ++i) af[i] = *(LAS v4u*)(ab + 512 * i);
#pragma unroll
      for (int i = 1; i < 8; ++i) bf[i] = *(LAS v4u*)(bb + 512 * (7 - i));
      SCHED();
#pragma unroll
      for (int ip = 0; ip < 8; ++ip) {
#pragma unroll
        for (int i = ip; i < 8; ++i) acc[i] = mfma16(af[i - ip], bf[ip], acc[i]);
        SCHED();
        ag[ip] = *(LAS v4u*)(ab - 64 + 512 * ip);
        bg[ip] = *(LAS v4u*)(bb - 64 + 512 * (7 - ip));
        SCHED();
      }
      lds_wait(L, 0);
      asm volatile("global_load_dwordx4 %0, %1, off sc1" : "=v"(xt) : "v"((const GAS char*)p.ws + OFF_XT + (bch * S + s0) * 2) : "memory");
      dma2s((const GAS char*)p.ws + OFF_ST + w * 2048 + lane * 16, LB + B_ST + w * 2048);
      SCHED();
#pragma unroll
      for (int i = 0; i < 8; ++i) {
#pragma unroll
        for (int ip = 0; ip <= i; ++ip) acc[i] = mfma16(ag[i - ip], bg[ip], acc[i]);
        SCHED();
        if (i > 0) red[(w * 8 + i - 1) * 64 + lane] = acc[i - 1];
        SCHED();
      }
      red[(w * 8 + 7) * 64 + lane] = acc[7];
    }
    STAMPT(21);
    asm volatile("s_waitcnt vmcnt(0)" : "+v"(xt) :: "memory");
    BARRIER();
    if (tid < 8) st4(tag, rs, OFF_FA3 + tid * 4096 + bid * 16);
    STAMPT(6);
    f32x2 ya[2], yb[2];
    {
      LAS f32x4* rp = red + (chl * 32 + 2 * q4) * 64 + lane;
      const f32x4 a0 = rp[0], a1 = rp[8 * 64], a2 = rp[16 * 64], a3 = rp[24 * 64];
      const f32x4 b0 = rp[64], b1 = rp[9 * 64], b2 = rp[17 * 64], b3 = rp[25 * 64];
      ya[0] = (a0.xy + a1.xy) + (a2.xy + a3.xy); ya[1] = (a0.zw + a1.zw) + (a2.zw + a3.zw);
      yb[0] = (b0.xy + b1.xy) + (b2.xy + b3.xy); yb[1] = (b0.zw + b1.zw) + (b2.zw + b3.zw);
    }
    const v4u kvr = *(LAS v4u*)(L + B_XC + (chl * 2 + 1) * B_XCS + 2 * (S - 8 - s0));
    const unsigned kvp[4] = {kvr.w, kvr.z, kvr.y, kvr.x};
    const unsigned xp[4] = {xt.x, xt.y, xt.z, xt.w};
    unsigned ho[4];
#pragma unroll
    for (int r = 0; r < 4; ++r) {
      const f32x4 st = *(LAS f32x4*)(L + B_ST + 8 * s0 + 16 * r);
      const float va = ya[r >> 1][r & 1], vb = yb[r >> 1][r & 1];
      const float snd = al ? va : vb;
      const float rcv = asf(__builtin_amdgcn_update_dpp(0, asu(snd), 0xB1, 0xf, 0xf, true));
      const f32x2 y = {al ? rcv : va, al ? vb : rcv};
      const f32x2 xx = up2(xp[r]);
      const f32x2 kv = {bfhi(kvp[r]), bflo(kvp[r])};
      const f32x2 t = fma2(kv, f32x2{dch, dch}, y);
      const f32x2 qn = fma2(xx, st.yw, st.xz);
      const f32x2 h = fma2(qn, t, xx);
      ho[r] = pk(h.x, h.y);
    }
    {
      const int sl = (s0 & 63) >> 3;
      st16(v4u{ho[0], ho[1], ho[2], ho[3]}, rs, OFF_HT + ((s0 >> 6) * D + bch) * 128 + ((sl ^ (((bch >> 1) & 1) << 2)) << 4));
    }
    STAMPT(7);
    if (w == 0) poll4(rs, OFF_FA3 + xcd * 4096 + lane * 16, tag);
    WAIT_V(0);
    BARRIER();
    if (tid < 8) st4(tag, rs, OFF_FB + tid * 4096 + bid * 16);
    STAMPT(8);
  }

  {
    if (w == 0) poll4(rs, OFF_FB + xcd * 4096 + lane * 16, tag);
#pragma unroll
    for (int gi = 0; gi < 4; ++gi) {
      const unsigned vo = OFF_W1 + ((d_nt * 8 + w) * 16 + ((gi + nq) & 3) * 4) * 1024 + lane * 16;
      GLD(wf1[4 * gi], vo, p.ws, 0); GLD(wf1[4 * gi + 1], vo, p.ws, 1024); GLD(wf1[4 * gi + 2], vo, p.ws, 2048); GLD(wf1[4 * gi + 3], vo, p.ws, 3072);
    }
    asm volatile("global_load_dwordx4 %0, %1, %2" : "=v"(b1a) : "v"((unsigned)(128 * d_nt + 32 * nq + 8 * hh) * 2), "s"(p.b1) : "memory");
    asm volatile("global_load_dwordx4 %0, %1, %2 offset:32" : "=v"(b1b) : "v"((unsigned)(128 * d_nt + 32 * nq + 8 * hh) * 2), "s"(p.b1) : "memory");
    SCHED();
    BARRIER();
    STAMPT(9);
    if (d_act) {
      const GAS unsigned char* src = (const GAS unsigned char*)p.ws + OFF_HT + (size_t)(2 * d_mt) * 65536 + w * 8192 + lane * 16;
#pragma unroll
      for (int c = 0; c < 2; ++c)
#pragma unroll
        for (int i = 0; i < 2; ++i) dma4s(src + c * 65536 + i * 4096, LB + D_TILE + c * 65536 + w * 8192 + i * 4096);
    }
    {
      const int rr = 16 * (w & 3) + (lane >> 2);
      const int dch2 = 64 * e_dt + rr;
      dma1s((const GAS unsigned char*)p.ws + OFF_HT + ((2 * e_mt + (w >> 2)) * D + dch2) * 128 + (((unsigned)e_j ^ ((dch2 >> 1) & 1)) << 6) + 16 * (lane & 3),
            LB + E_RES + w * 1024);
    }
    SCHED();
    const unsigned w2off = OFF_W2 + (e_dt * 8 + w) * 28 * 1024 + lane * 16;
    if (d_act) {
      asm volatile("s_waitcnt vmcnt(17)" : "+v"(wf1[0]), "+v"(wf1[1]), "+v"(wf1[2]), "+v"(wf1[3]), "+v"(wf1[4]), "+v"(wf1[5]), "+v"(wf1[6]), "+v"(wf1[7]), "+v"(b1a) :: "memory");
      asm volatile("" : "+v"(wf1[8]), "+v"(wf1[9]), "+v"(wf1[10]), "+v"(wf1[11]), "+v"(wf1[12]), "+v"(wf1[13]), "+v"(wf1[14]), "+v"(wf1[15]), "+v"(b1b) :: "memory");
      {
        float c0 = 0.f, c1 = 0.f;
#pragma unroll
        for (int i = 0; i < 16; ++i) {
          c0 = dot2(wf1[i].x, ONES, c0); c1 = dot2(wf1[i].y, ONES, c1);
          c0 = dot2(wf1[i].z, ONES, c0); c1 = dot2(wf1[i].w, ONES, c1);
        }
        const float t = halfsum(c0 + c1);
        if (lane < 32) *(LAS float*)(L + D_CS + (w * 32 + lane) * 4) = t;
      }
      const unsigned basel = (unsigned)(D_TILE + (256 * kh + 8 * hh + tq) * 128 + 32 * blk + 8 * tp);
      const unsigned fsw = ((tq >> 1) & 1) << 6;
      const unsigned adr[2] = {basel + ((unsigned)(kh << 6) ^ fsw), basel + ((unsigned)((kh ^ 1) << 6) ^ fsw)};
      f32x16 acc[2][2];
      auto fraddr = [&](int c, int m) { return L + (adr[m & 1] + (unsigned)(c * 65536 + 8192 * ((((m >> 1) >> 2) + nq) & 3)) + 2048 * ((m >> 1) & 3)); };
      float xg[16];
      unsigned og[8];
      auto hstore = [&](int c) {
        const unsigned ho = OFF_HF + ((4 * d_mt + 2 * c + kh) * 112 + 8 * d_nt + 2 * nq) * 1024 + lane * 16;
        st16(v4u{og[0], og[1], og[2], og[3]}, rs, ho);
        st16(v4u{og[4], og[5], og[6], og[7]}, rs, ho + 1024);
      };
      float ge[16];
      auto gelu_1 = [&](int e) { const float x = xg[e]; ge[e] = __builtin_amdgcn_exp2f(x * __builtin_fmaf(x * x, -0.10294324f, -2.3022082f)); };
      auto gelu_2 = [&](int e) { ge[e] = __builtin_amdgcn_rcpf(ge[e] + 1.0f); };
      auto gelu_3 = [&](int e) {
        float y = xg[e] * ge[e];
        asm("" : "+v"(y));
        xg[e] = y;
        if (e & 1) og[e >> 1] = pk(xg[e - 1], xg[e]);
      };
      auto mstep = [&](int c) {
        float s1[2][2] = {{0.f, 0.f}, {0.f, 0.f}}, s2[2][2] = {{0.f, 0.f}, {0.f, 0.f}};
#pragma unroll
        for (int r = 0; r < 2; ++r)
#pragma unroll
          for (int e = 0; e < 16; ++e) acc[c][r][e] = 0.f;
        v4u fr[4];
#pragma unroll
        for (int n = 0; n < 3; ++n) fr[n] = trfrag(fraddr(c, n), 512);
        SCHED();
#pragma unroll
        for (int n = 0; n < 32; ++n) {
          const int i = n >> 1, r = n & 1;
          if (n + 3 < 32) fr[(n + 3) & 3] = trfrag(fraddr(c, n + 3), 512);
          acc[c][r] = mfma32(wf1[i], fr[n & 3], acc[c][r]);
          if (i < 4) {
            const v4u hf = fr[n & 3];
            s1[r][0] = dot2(hf.x, ONES, s1[r][0]); s2[r][0] = dot2(hf.x, hf.x, s2[r][0]);
            s1[r][1] = dot2(hf.y, ONES, s1[r][1]); s2[r][1] = dot2(hf.y, hf.y, s2[r][1]);
            s1[r][0] = dot2(hf.z, ONES, s1[r][0]); s2[r][0] = dot2(hf.z, hf.z, s2[r][0]);
            s1[r][1] = dot2(hf.w, ONES, s1[r][1]); s2[r][1] = dot2(hf.w, hf.w, s2[r][1]);
          }
          if (c == 1) {
            if (n >= 4 && n < 20) gelu_1(n - 4);
            if (n >= 6 && n < 22) gelu_2(n - 6);
            if (n >= 8 && n < 24) gelu_3(n - 8);
            if (n == 25) hstore(0);
          }
          if (n >= 18) wf2[14 * c + n - 18] = ld16(rs, w2off + (14 * c + n - 18) * 1024);
          SCHED();
        }
#pragma unroll
        for (int r = 0; r < 2; ++r) {
          const float a = halfsum(s1[r][0] + s1[r][1]), b = halfsum(s2[r][0] + s2[r][1]);
          if (lane < 32) *(LAS f32x2*)(L + D_STAT + (((c * 8 + w) * 2 + ((r + kh) & 1)) * 32 + lane) * 8) = f32x2{a, b};
        }
      };
      auto xwrite = [&](int c) {
        LAS f32x4* ex = (LAS f32x4*)(L + D_TILE + c * 32768) + w * 256 + lane;
#pragma unroll
        for (int qd = 0; qd < 4; ++qd) ex[qd * 64] = f32x4{acc[c][1][4 * qd], acc[c][1][4 * qd + 1], acc[c][1][4 * qd + 2], acc[c][1][4 * qd + 3]};
      };
      auto xpre = [&](int c) {
        LAS f32x4* ex = (LAS f32x4*)(L + D_TILE + c * 32768) + (w ^ 4) * 256 + lane;
        f32x2 sa = {0.f, 0.f};
#pragma unroll
        for (int ww = 0; ww < 8; ++ww) sa += *(LAS f32x2*)(L + D_STAT + (((c * 8 + ww) * 2 + kh) * 32 + (lane & 31)) * 8);
        const float mean = sa.x * (1.0f / D);
        float var = __builtin_fmaf(-mean, mean, sa.y * (1.0f / D));
        var = var > 0.f ? var : 0.f;
        const float r1 = __builtin_amdgcn_rsqf(var + 1e-5f);
        const float rstd = r1 * __builtin_amdgcn_rsqf(__builtin_fmaf(var * r1, r1, 1e-5f));
        const float nm = -rstd * mean;
        const f32x2 rs2 = {rstd, rstd}, nm2 = {nm, nm};
        LAS f32x4* c4 = (LAS f32x4*)(L + D_CS) + nq * 8 + hh;
        const unsigned bb[8] = {b1a.x, b1a.y, b1a.z, b1a.w, b1b.x, b1b.y, b1b.z, b1b.w};
#pragma unroll
        for (int qd = 0; qd < 4; ++qd) {
          const f32x4 pp = ex[qd * 64];
          const f32x4 cs = c4[2 * qd] + c4[32 + 2 * qd];
#pragma unroll
          for (int hf = 0; hf < 2; ++hf) {
            const int e = 2 * qd + hf;
            const f32x2 a = f32x2{acc[c][0][2 * e], acc[c][0][2 * e + 1]} + (hf ? pp.zw : pp.xy);
            const f32x2 x = fma2(rs2, a, fma2(nm2, hf ? cs.zw : cs.xy, up2(bb[e])));
            xg[2 * e] = x.x; xg[2 * e + 1] = x.y;
            if (c == 0) { asm("" : "+v"(xg[2 * e])); asm("" : "+v"(xg[2 * e + 1])); }
          }
        }
      };
      auto gelu_all = [&]() {
#pragma unroll
        for (int e = 0; e < 8; ++e) {
          const f32x2 x = {xg[2 * e], xg[2 * e + 1]};
          const f32x2 pz = fma2(x * x, f32x2{-0.10294324f, -0.10294324f}, f32x2{-2.3022082f, -2.3022082f});
          const f32x2 z = x * pz;
          const f32x2 den = f32x2{__builtin_amdgcn_exp2f(z.x), __builtin_amdgcn_exp2f(z.y)} + f32x2{1.f, 1.f};
          const f32x2 y = x * f32x2{__builtin_amdgcn_rcpf(den.x), __builtin_amdgcn_rcpf(den.y)};
          og[e] = pk(y.x, y.y);
        }
      };
      WAIT_V(9);
      BARRIER();
      STAMPT(10);
      SCHED();
      mstep(0);
      STAMPT(22);
      WAIT_V(14);
      BARRIER();
      STAMPT(11);
      xwrite(0);
      BARRIER();
      STAMPT(23);
      xpre(0);
      STAMPT(24);
      mstep(1);
      STAMPT(25);
      xwrite(1);
      BARRIER();
      STAMPT(12);
      xpre(1);
      WAIT_V(7);
      lds_count_flag(L, 2, 8, tid, d_act, tag, rs, OFF_FD + d_mt * 256 + d_nt * 16);
      gelu_all();
      hstore(1);
      STAMPT(13);
    } else {
#pragma unroll
      for (int i = 0; i < 28; ++i) wf2[i] = ld16(rs, w2off + i * 1024);
    }
  }

  {
    const int sbo = w >> 2, dbo = (w >> 1) & 1, hf = w & 1;
    const v4u b2v = *(const GAS v4u*)((const GAS char*)p.b2 + (unsigned)(64 * e_dt + 32 * dbo + 16 * hf + 8 * hh) * 2);
    if (w == 0) poll1(rs, OFF_FD + e_mt * 256 + (lane < 14 ? lane : 0) * 16, tag);
    WAIT_V(0);
    BARRIER();
    if (d_act && tid == 0) st4(tag, rs, OFF_FD + 4096 + d_mt * 256 + d_nt * 16);
    STAMPT(14);
    const unsigned hoff0 = OFF_HF + ((4 * e_mt + e_j) * 112 + 14 * w) * 1024 + lane * 16, hoff1 = hoff0 + 2 * 112 * 1024;
    if (w == 0) { poll1(rs, OFF_FD + 4096 + e_mt * 256 + (lane < 14 ? lane : 0) * 16, tag); lds_set(L, 1); }
    v4u hf0[14], hf1[14];
#pragma unroll
    for (int k = 0; k < 14; ++k) hf0[k] = ld16(rs, hoff0 + k * 1024);
    SCHED();
    lds_wait(L, 1);
    STAMPT(15);
    LAS f32x4* red = (LAS f32x4*)(L + E_RED + w * 16384) + lane;
    {
      f32x16 acc[2];
#pragma unroll
      for (int e = 0; e < 16; ++e) { acc[0][e] = 0.f; acc[1][e] = 0.f; }
#pragma unroll
      for (int k = 0; k < 14; ++k) {
        acc[0] = mfma32(wf2[2 * k], hf0[k], acc[0]); acc[1] = mfma32(wf2[2 * k + 1], hf0[k], acc[1]);
        SCHED();
        hf1[k] = ld16(rs, hoff1 + k * 1024);
        SCHED();
      }
#pragma unroll
      for (int d = 0; d < 2; ++d)
#pragma unroll
        for (int qd = 0; qd < 4; ++qd) red[(d * 4 + qd) * 64] = f32x4{acc[d][4 * qd], acc[d][4 * qd + 1], acc[d][4 * qd + 2], acc[d][4 * qd + 3]};
    }
    STAMPT(16);
    {
      f32x16 acc[2];
#pragma unroll
      for (int e = 0; e < 16; ++e) { acc[0][e] = 0.f; acc[1][e] = 0.f; }
#pragma unroll
      for (int k = 0; k < 14; ++k) { acc[0] = mfma32(wf2[2 * k], hf1[k], acc[0]); acc[1] = mfma32(wf2[2 * k + 1], hf1[k], acc[1]); }
#pragma unroll
      for (int d = 0; d < 2; ++d)
#pragma unroll
        for (int qd = 0; qd < 4; ++qd) red[512 + (d * 4 + qd) * 64] = f32x4{acc[d][4 * qd], acc[d][4 * qd + 1], acc[d][4 * qd + 2], acc[d][4 * qd + 3]};
    }
    BARRIER();
    STAMPT(17);
    f32x2 o[4] = {{0.f, 0.f}, {0.f, 0.f}, {0.f, 0.f}, {0.f, 0.f}};
#pragma unroll
    for (int ww = 0; ww < 8; ++ww) {
      LAS f32x4* rp = (LAS f32x4*)(L + E_RED + ww * 16384) + sbo * 512 + (dbo * 4 + 2 * hf) * 64 + lane;
      const f32x4 a = rp[0], b = rp[64];
      o[0] += a.xy; o[1] += a.zw; o[2] += b.xy; o[3] += b.zw;
    }
    const v4u hr = trfrag(L + E_RES + sbo * 4096 + (32 * dbo + 16 * hf + 8 * hh + tq) * 64 + 32 * blk + 8 * tp, 256);
    o[0] += up2(b2v.x) + up2(hr.x); o[1] += up2(b2v.y) + up2(hr.y);
    o[2] += up2(b2v.z) + up2(hr.z); o[3] += up2(b2v.w) + up2(hr.w);
    const v4u ov = {pk(o[0].x, o[0].y), pk(o[1].x, o[1].y), pk(o[2].x, o[2].y), pk(o[3].x, o[3].y)};
    *(GAS v4u*)((GAS char*)p.out + (unsigned)((128 * e_mt + 64 * sbo + 32 * e_j + (lane & 31)) * D + 64 * e_dt + 32 * dbo + 16 * hf + 8 * hh) * 2) = ov;
    if (bid == 0 && tid == 0) st4(tag, rs, OFF_EP);
    STAMPT(18);
  }
}

extern "C" void kernel_launch(void* const* d_in, const int* in_sizes, int n_in, void* d_out, int out_size, void* d_ws,
                              size_t ws_size, hipStream_t stream) {
  Params p;
  p.q = (const u16*)d_in[0]; p.k = (const u16*)d_in[1]; p.v = (const u16*)d_in[2];
  p.shk = (const u16*)d_in[3]; p.ssk = (const u16*)d_in[4]; p.ssd = (const u16*)d_in[5];
  p.w1 = (const u16*)d_in[7]; p.b1 = (const u16*)d_in[8]; p.w2 = (const u16*)d_in[9]; p.b2 = (const u16*)d_in[10];
  p.out = (u16*)d_out; p.ws = (unsigned char*)d_ws;
  fused<<<256, 512, 0, stream>>>(p);
}
```

```cpp
#include <hip/hip_runtime.h>
#include <hip/hip_bf16.h>

#define GAS __attribute__((address_space(1)))
#define LAS __attribute__((address_space(3)))
typedef unsigned short u16;
typedef unsigned v4u __attribute__((ext_vector_type(4)));
typedef unsigned v2u __attribute__((ext_vector_type(2)));
typedef float f32x16 __attribute__((ext_vector_type(16)));
typedef float f32x4 __attribute__((ext_vector_type(4)));
typedef float f32x2 __attribute__((ext_vector_type(2)));
typedef short s16x4 __attribute__((ext_vector_type(4)));
typedef short bf16x8 __attribute__((ext_vector_type(8)));
typedef __bf16 bf2 __attribute__((ext_vector_type(2)));
typedef __amdgpu_buffer_rsrc_t rsrc_t;

constexpr int S = 2048, D = 512, F = 1792;
constexpr unsigned OFF_EP = 0;
constexpr unsigned OFF_FA1 = 4096;
constexpr unsigned OFF_FD = 8192;
constexpr unsigned OFF_FB0 = 16384;
constexpr unsigned OFF_FB1 = OFF_FB0 + 32768;
constexpr unsigned OFF_XR = 262144;
constexpr unsigned OFF_XT = OFF_XR + D * S * 2;
constexpr unsigned OFF_HT = OFF_XT + D * S * 2;
constexpr unsigned OFF_ST = OFF_HT + D * S * 2;
constexpr unsigned OFF_W1 = OFF_ST + 16384;
constexpr unsigned OFF_W2 = OFF_W1 + D * F * 2;
constexpr unsigned OFF_HF = OFF_W2 + D * F * 2;
constexpr unsigned WS_END = OFF_HF + S * F * 2;
constexpr int SPIN_CAP = 1 << 16;

struct Params {
  const u16 *q, *k, *v, *shk, *ssk, *ssd, *w1, *b1, *w2, *b2;
  u16* out;
  unsigned char* ws;
};

__device__ __forceinline__ float asf(unsigned u) { return __builtin_bit_cast(float, u); }
__device__ __forceinline__ unsigned asu(float f) { return __builtin_bit_cast(unsigned, f); }
__device__ __forceinline__ float bflo(unsigned u) { return asf(u << 16); }
__device__ __forceinline__ float bfhi(unsigned u) { return asf(u & 0xffff0000u); }
__device__ __forceinline__ unsigned pk(float lo, float hi) {
  f32x2 f = {lo, hi};
  return __builtin_bit_cast(unsigned, __builtin_convertvector(f, bf2));
}
__device__ __forceinline__ f32x2 up2(unsigned u) { return f32x2{bflo(u), bfhi(u)}; }
__device__ __forceinline__ f32x2 fma2(f32x2 a, f32x2 b, f32x2 c) { return __builtin_elementwise_fma(a, b, c); }
__device__ __forceinline__ f32x4 mfma16(v4u a, v4u b, f32x4 c) {
  return __builtin_amdgcn_mfma_f32_16x16x32_bf16(__builtin_bit_cast(bf16x8, a), __builtin_bit_cast(bf16x8, b), c, 0, 0, 0);
}
__device__ __forceinline__ f32x16 mfma32(v4u a, v4u b, f32x16 c) {
  return __builtin_amdgcn_mfma_f32_32x32x16_bf16(__builtin_bit_cast(bf16x8, a), __builtin_bit_cast(bf16x8, b), c, 0, 0, 0);
}
__device__ __forceinline__ v2u trread(LAS unsigned char* p) {
  return __builtin_bit_cast(v2u, __builtin_amdgcn_ds_read_tr16_b64_v4i16((LAS s16x4*)p));
}
__device__ __forceinline__ v4u trfrag(LAS unsigned char* p, int second) {
  const v2u a = trread(p), b = trread(p + second);
  return v4u{a.x, a.y, b.x, b.y};
}
__device__ __forceinline__ float dot2(unsigned a, unsigned b, float c) {
  return __builtin_amdgcn_fdot2_f32_bf16(__builtin_bit_cast(bf2, a), __builtin_bit_cast(bf2, b), c, false);
}
constexpr int SC1 = 16;
#define DMA_HEAD "s_mov_b32 m0, %1\n\ts_nop 0\n\t"
#define DMA_P(o, f) "global_load_lds_dwordx4 %0, off offset:" #o f "\n\t"
__device__ __forceinline__ void dma4(const GAS void* g, unsigned lds) {
  asm volatile(DMA_HEAD DMA_P(0, "") DMA_P(1024, "") DMA_P(2048, "") DMA_P(3072, "") :: "v"(g), "s"(lds) : "memory");
}
__device__ __forceinline__ void dma3(const GAS void* g, unsigned lds) {
  asm volatile(DMA_HEAD DMA_P(0, "") DMA_P(1024, "") DMA_P(2048, "") :: "v"(g), "s"(lds) : "memory");
}
__device__ __forceinline__ void dma1(const GAS void* g, unsigned lds) {
  asm volatile(DMA_HEAD DMA_P(0, "") :: "v"(g), "s"(lds) : "memory");
}
__device__ __forceinline__ void dma4s(const GAS void* g, unsigned lds) {
  asm volatile(DMA_HEAD DMA_P(0, " sc1") DMA_P(1024, " sc1") DMA_P(2048, " sc1") DMA_P(3072, " sc1") :: "v"(g), "s"(lds) : "memory");
}
__device__ __forceinline__ void dma3s(const GAS void* g, unsigned lds) {
  asm volatile(DMA_HEAD DMA_P(0, " sc1") DMA_P(1024, " sc1") DMA_P(2048, " sc1") :: "v"(g), "s"(lds) : "memory");
}
__device__ __forceinline__ void dma2s(const GAS void* g, unsigned lds) {
  asm volatile(DMA_HEAD DMA_P(0, " sc1") DMA_P(1024, " sc1") :: "v"(g), "s"(lds) : "memory");
}
__device__ __forceinline__ void dma1s(const GAS void* g, unsigned lds) {
  asm volatile(DMA_HEAD DMA_P(0, " sc1") :: "v"(g), "s"(lds) : "memory");
}
__device__ __forceinline__ v4u ld16(rsrc_t rs, unsigned off) { return __builtin_amdgcn_raw_buffer_load_b128(rs, off, 0, SC1); }
__device__ __forceinline__ void st16(v4u v, rsrc_t rs, unsigned off) { __builtin_amdgcn_raw_buffer_store_b128(v, rs, off, 0, SC1); }
__device__ __forceinline__ void st4(unsigned v, rsrc_t rs, unsigned off) { __builtin_amdgcn_raw_buffer_store_b32(v, rs, off, 0, SC1); }
template <int CTRL, int RM>
__device__ __forceinline__ float dppadd(float x) {
  return x + asf(__builtin_amdgcn_update_dpp(0, asu(x), CTRL, RM, 0xf, true));
}
__device__ __forceinline__ float wavesum(float x) {
  x = dppadd<0x111, 0xf>(x); x = dppadd<0x112, 0xf>(x); x = dppadd<0x114, 0xf>(x); x = dppadd<0x118, 0xf>(x);
  x = dppadd<0x142, 0xa>(x); x = dppadd<0x143, 0xc>(x);
  return x;
}
__device__ __forceinline__ float halfsum(float x) {
  const unsigned u = asu(x);
  auto r = __builtin_amdgcn_permlane32_swap(u, u, false, false);
  const unsigned r0 = r[0], r1 = r[1];
  return asf(r0) + asf(r1);
}
#define WAIT_V(n) asm volatile("s_waitcnt vmcnt(" #n ")" ::: "memory")
#define BARRIER() do { asm volatile("s_waitcnt lgkmcnt(0)" ::: "memory"); __builtin_amdgcn_s_barrier(); asm volatile("" ::: "memory"); } while (0)
#define SCHED() __builtin_amdgcn_sched_barrier(0)
constexpr unsigned ONES = 0x3f803f80u;

#define STAMPT(idx)

#define CODEPF 0
constexpr unsigned CODE_BYTES = 22016;
constexpr int LDS_BYTES = 163840;
constexpr int A_K = 0, A_V = A_K + 68 * 128, A_Q = A_V + 8192, A_END = A_Q + 8192;
constexpr int B_HCS = 4160, B_XCS = 4640;
constexpr int L_FLAG = LDS_BYTES - 64;
constexpr int B_XC = L_FLAG - 4 * B_XCS, B_HC = B_XC - 8 * B_HCS;
constexpr int B_RED = 0, B_ST = 65536, A_W1 = B_ST + 16384, A_W2 = A_W1 + 8192;
constexpr int D_TILE = 0, D_STAT = 131072, D_CS = D_STAT + 2 * 8 * 64 * 8, D_END = D_CS + 1024;
constexpr int E_RED = 0, E_RES = 143360;
static_assert(A_W2 + 8192 <= B_HC && A_END <= 65536 && B_HC % 16 == 0 && D_END <= E_RES && E_RES + 8192 <= L_FLAG, "");

__device__ __forceinline__ void poll4(rsrc_t rs, unsigned off, unsigned tag) {
  for (int spins = 0; spins < SPIN_CAP; ++spins) {
    const unsigned f0 = __builtin_amdgcn_raw_buffer_load_b32(rs, off, 0, SC1), f1 = __builtin_amdgcn_raw_buffer_load_b32(rs, off + 1024, 0, SC1);
    const unsigned f2 = __builtin_amdgcn_raw_buffer_load_b32(rs, off + 2048, 0, SC1), f3 = __builtin_amdgcn_raw_buffer_load_b32(rs, off + 3072, 0, SC1);
    if (__builtin_amdgcn_ballot_w64(f0 == tag && f1 == tag && f2 == tag && f3 == tag) == ~0ull) break;
    __builtin_amdgcn_s_sleep(1);
    asm volatile("" ::: "memory");
  }
}
__device__ __forceinline__ void poll1(rsrc_t rs, unsigned off, unsigned tag) {
  for (int spins = 0; spins < SPIN_CAP; ++spins) {
    const unsigned f = __builtin_amdgcn_raw_buffer_load_b32(rs, off, 0, SC1);
    if (__builtin_amdgcn_ballot_w64(f == tag) == ~0ull) break;
    __builtin_amdgcn_s_sleep(1);
    asm volatile("" ::: "memory");
  }
}
__device__ __forceinline__ void lds_set(LAS unsigned char* L, int idx) {
  asm volatile("" ::: "memory");
  *(volatile LAS unsigned*)(L + L_FLAG + 4 * idx) = 1u;
}
__device__ __forceinline__ void lds_wait(LAS unsigned char* L, int idx) {
  for (int spins = 0; spins < (SPIN_CAP << 4); ++spins) {
    if (__builtin_amdgcn_readfirstlane(*(volatile LAS unsigned*)(L + L_FLAG + 4 * idx)) != 0) break;
    __builtin_amdgcn_s_sleep(1);
  }
  asm volatile("" ::: "memory");
}
#define GLD(dst, voff, base, imm) asm volatile("global_load_dwordx4 %0, %1, %2 offset:" #imm " sc1" : "=v"(dst) : "v"(voff), "s"(base) : "memory")
__device__ __forceinline__ void lds_count_flag(LAS unsigned char* L, int idx, unsigned n, int tid, bool act, unsigned tag, rsrc_t rs, unsigned off) {
  if ((tid & 63) == 0) {
    const unsigned old = __hip_atomic_fetch_add((LAS unsigned*)(L + L_FLAG + 4 * idx), 1u, __ATOMIC_RELAXED, __HIP_MEMORY_SCOPE_WORKGROUP);
    if (old == n - 1 && act) st4(tag, rs, off);
  }
}
__device__ __forceinline__ bool lds_count_flag8(LAS unsigned char* L, int idx, int lane, unsigned tag, rsrc_t rs, unsigned off) {
  unsigned old = 0;
  if (lane == 0) old = __hip_atomic_fetch_add((LAS unsigned*)(L + L_FLAG + 4 * idx), 1u, __ATOMIC_RELAXED, __HIP_MEMORY_SCOPE_WORKGROUP);
  const bool last = __builtin_amdgcn_readfirstlane(old) == 7;
  if (last && lane < 8) st4(tag, rs, off + lane * 4096);
  return last;
}
__device__ __forceinline__ void poll5(rsrc_t rs, unsigned offa, unsigned off, unsigned tag) {
  for (int spins = 0; spins < SPIN_CAP; ++spins) {
    const unsigned fa = __builtin_amdgcn_raw_buffer_load_b32(rs, offa, 0, SC1);
    const unsigned f0 = __builtin_amdgcn_raw_buffer_load_b32(rs, off, 0, SC1), f1 = __builtin_amdgcn_raw_buffer_load_b32(rs, off + 1024, 0, SC1);
    const unsigned f2 = __builtin_amdgcn_raw_buffer_load_b32(rs, off + 2048, 0, SC1), f3 = __builtin_amdgcn_raw_buffer_load_b32(rs, off + 3072, 0, SC1);
    if (__builtin_amdgcn_ballot_w64(fa == tag && f0 == tag && f1 == tag && f2 == tag && f3 == tag) == ~0ull) break;
    __builtin_amdgcn_s_sleep(1);
    asm volatile("" ::: "memory");
  }
}
#define LOOK_ISSUE(off) do { const GAS char* fp_ = (const GAS char*)p.ws + (off); \
    asm volatile("global_load_dword %0, %1, off sc1" : "=v"(pf0) : "v"(fp_) : "memory"); \
    asm volatile("global_load_dword %0, %1, off offset:1024 sc1" : "=v"(pf1) : "v"(fp_) : "memory"); \
    asm volatile("global_load_dword %0, %1, off offset:2048 sc1" : "=v"(pf2) : "v"(fp_) : "memory"); \
    asm volatile("global_load_dword %0, %1, off offset:3072 sc1" : "=v"(pf3) : "v"(fp_) : "memory"); } while (0)
#define LOOK_CHECK(n, off) do { asm volatile("s_waitcnt vmcnt(" #n ")" : "+v"(pf0), "+v"(pf1), "+v"(pf2), "+v"(pf3) :: "memory"); \
    if (__builtin_amdgcn_ballot_w64(pf0 == tag && pf1 == tag && pf2 == tag && pf3 == tag) != ~0ull) poll4(rs, (off), tag); } while (0)
#define WAITF(n, x) asm volatile("s_waitcnt vmcnt(" #n ")" : "+v"(x) :: "memory")
#define ALOAD(op, dst, ptr) asm volatile(op " %0, %1, off" : "=v"(dst) : "v"(ptr) : "memory")

__global__ void __launch_bounds__(512, 2) fused(Params p) {
  __shared__ __attribute__((aligned(16))) unsigned char smem[LDS_BYTES];
  LAS unsigned char* L = (LAS unsigned char*)smem;
  const unsigned LB = (unsigned)(size_t)L;
  const int tid = threadIdx.x, lane = tid & 63, w = __builtin_amdgcn_readfirstlane(tid >> 6);
  const int bid = blockIdx.x, xcd = bid & 7, bix = bid >> 3;
  const rsrc_t rs = __builtin_amdgcn_make_buffer_rsrc((void*)p.ws, 0, (int)WS_END, 0x00020000);
  const int g = lane >> 4, i16 = lane & 15, tq = i16 >> 2, tp = i16 & 3;
  const int hh = lane >> 5, blk = g & 1;
  STAMPT(0);

  const int cj = xcd * 32 + bix;
  const bool d_act = bix < 28;
  const int d_mt = 4 * (xcd >> 1) + (d_act ? bix / 7 : 0), d_nt = 7 * (xcd & 1) + bix % 7;
  const int e_mt = 2 * xcd + (bix >> 4), e_j = (bix >> 3) & 1, e_dt = bix & 7;
  const int chl = w >> 2, q4 = w & 3, bch = 2 * cj + chl;

  v4u wf1[16];
  unsigned tag;
  unsigned hval, dv;
  {
    const int b = xcd, a = bix;
    const int ch = 64 * b + 16 * (w & 3) + i16, seg = 4 * (w >> 2) + g;
    const int srow = 64 * a + 8 * b + w;
    v2u shw; v4u qs;
    {
      const int rl = lane >> 3, sl = lane & 7;
      const int rhok = 4 + 8 * w + rl, rhov = 8 * w + rl;
      const unsigned fk = ((rhok >> 1) & 1) | (((rhok >> 3) & 1) << 1), fv = ((rhov >> 1) & 1) | (((rhov >> 3) & 1) << 1);
      const unsigned rowoff = ((64 * a + 8 * w + rl) * D + 64 * b) * 2 + 16 * (sl & 1);
      const unsigned uk = rowoff + 32 * ((sl >> 1) ^ fk), uv = rowoff + 32 * ((sl >> 1) ^ fv);
      if (w == 0 && a > 0 && lane < 32)
        dma1((const GAS char*)p.k + (((64 * a - 4 + rl) * D + 64 * b) * 2 + 16 * (sl & 1) + 32 * ((sl >> 1) ^ ((rl >> 1) & 1))), LB + A_K);
      ALOAD("global_load_dwordx4", qs, (const GAS char*)p.q + (unsigned)(srow * D + 8 * lane) * 2);
      asm volatile("global_load_dword %0, %1, off sc1" : "=v"(tag) : "v"((const GAS char*)p.ws + OFF_EP) : "memory");
      ALOAD("global_load_dwordx2", shw, (const GAS char*)p.shk + (unsigned)ch * 8);
      {
        const int hidx = (lane & 31) + 2 * q4 - 30;
        ALOAD("global_load_ushort", hval, (const GAS u16*)(p.ssk + (size_t)bch * S + (hidx < 0 ? 0 : hidx)));
        ALOAD("global_load_ushort", dv, (const GAS u16*)(p.ssd + bch));
      }
      dma1((const GAS char*)p.k + uk, LB + A_K + 512 + w * 1024);
      dma1((const GAS char*)p.v + uv, LB + A_V + w * 1024);
      dma1((const GAS char*)p.q + uv, LB + A_Q + w * 1024);
    }
    if (a == 0 && tid < 32) *(LAS v4u*)(L + A_K + 16 * tid) = v4u{0, 0, 0, 0};
    if (tid < 16) *(LAS unsigned*)(L + L_FLAG + 4 * tid) = 0u;
    if (tid < 128) *(LAS v4u*)(L + B_XC + (tid >> 5) * B_XCS + 4096 + 16 * (tid & 31)) = v4u{0, 0, 0, 0};
    asm volatile("s_waitcnt vmcnt(3)" : "+v"(qs), "+v"(tag), "+v"(shw), "+v"(hval), "+v"(dv) :: "memory");
    tag += 1u;
    {
      float s1 = dot2(qs.x, ONES, dot2(qs.y, ONES, 0.f)) + dot2(qs.z, ONES, dot2(qs.w, ONES, 0.f));
      float s2 = dot2(qs.x, qs.x, dot2(qs.y, qs.y, 0.f)) + dot2(qs.z, qs.z, dot2(qs.w, qs.w, 0.f));
      s1 = wavesum(s1); s2 = wavesum(s2);
      const float mean = s1 * (1.0f / D);
      float var = __builtin_fmaf(-mean, mean, s2 * (1.0f / D));
      var = var > 0.f ? var : 0.f;
      const float rstd = __builtin_amdgcn_rsqf(var + 1e-5f);
      if (lane == 63) __builtin_amdgcn_raw_buffer_store_b64(v2u{pk(-mean * rstd, rstd), tag}, rs, OFF_ST + 8 * srow, 0, SC1);
    }
    WAIT_V(1);
    BARRIER();
    STAMPT(1);
    const int s0 = 64 * a + 8 * seg;
    const int r0 = 8 * seg + tq;
    const unsigned fr0 = (r0 >> 1) & 1, sg = seg & 1;
    const unsigned ua = ((w & 3) ^ (fr0 | (sg << 1))) * 32 + 8 * tp, ub = ((w & 3) ^ (fr0 | ((sg ^ 1) << 1))) * 32 + 8 * tp;
    {
      LAS unsigned char* kb = L + A_K + r0 * 128;
      const v2u k0 = trread(kb + ua), k1 = trread(kb + 512 + ua), k2 = trread(kb + 1024 + ub);
      LAS unsigned char* vb = L + A_V + r0 * 128 + ua;
      const v2u v0 = trread(vb), v1 = trread(vb + 512);
      LAS unsigned char* qb = L + A_Q + r0 * 128 + ua;
      const v2u q0 = trread(qb), q1 = trread(qb + 512);
      const unsigned E[6] = {k0.x, k0.y, k1.x, k1.y, k2.x, k2.y};
      unsigned O[5];
#pragma unroll
      for (int i = 0; i < 5; ++i) O[i] = __builtin_amdgcn_alignbit(E[i + 1], E[i], 16);
      const unsigned shx = shw.x, shy = shw.y;
      const unsigned w32 = __builtin_amdgcn_alignbit(shy, shy, 16);
      const unsigned w10 = __builtin_amdgcn_alignbit(shx, shx, 16);
      float ks[8];
#pragma unroll
      for (int j = 0; j < 8; ++j) {
        const unsigned pa = (j & 1) ? E[(j + 1) >> 1] : O[j >> 1];
        const unsigned pb = (j & 1) ? E[(j + 3) >> 1] : O[(j + 2) >> 1];
        ks[j] = dot2(pb, w10, dot2(pa, w32, 0.f));
      }
      const f32x2 a0 = f32x2{ks[0], ks[1]} * up2(v0.x), a1 = f32x2{ks[2], ks[3]} * up2(v0.y);
      const f32x2 a2 = f32x2{ks[4], ks[5]} * up2(v1.x), a3 = f32x2{ks[6], ks[7]} * up2(v1.y);
      const v4u xr = {pk(a3.y, a3.x), pk(a2.y, a2.x), pk(a1.y, a1.x), pk(a0.y, a0.x)};
      st16(xr, rs, OFF_XR + (ch * S + (S - 8 - s0)) * 2);
      st16(v4u{q0.x, q0.y, q1.x, q1.y}, rs, OFF_XT + (ch * S + s0) * 2);
    }
    STAMPT(2);
    WAIT_V(0);
    BARRIER();
    if (tid == 0) st4(tag, rs, OFF_FA1 + (b * 32 + a) * 16);
    STAMPT(3);
    if (w >= 4) {
      const int w4 = w - 4, wb = bid < 224 ? bid : 0;
#pragma unroll
      for (int cc = 0; cc < 2; ++cc) {
        const int cw = 2 * w4 + cc;
        const u16* hrow = p.ssk + (size_t)(2 * cj + (cw >> 2)) * S;
        const int idx = 8 * (4 + lane) + 2 * (cw & 3) - 30;
        const unsigned lb = LB + B_HC + cw * B_HCS + 64;
        dma3((const GAS void*)(hrow + idx), lb);
        int idx2 = idx + 3 * 512;
        idx2 = idx2 > S - 8 ? S - 8 : idx2;
        dma1((const GAS void*)(hrow + idx2), lb + 3072);
      }
#pragma unroll
      for (int cc = 0; cc < 2; ++cc) {
        const int wr = 16 * cc + (lane >> 2);
        dma1((const GAS char*)p.w1 + (unsigned)((32 * (wb / 14) + wr) * F + 128 * (wb % 14) + 32 * w4 + 8 * (lane & 3)) * 2, LB + A_W1 + w4 * 2048 + cc * 1024);
        dma1((const GAS char*)p.w2 + (unsigned)((32 * (wb >> 2) + wr) * D + 128 * (wb & 3) + 32 * w4 + 8 * (lane & 3)) * 2, LB + A_W2 + w4 * 2048 + cc * 1024);
      }
    }
    if (w == 0) poll1(rs, OFF_FA1 + (xcd * 32 + (lane & 31)) * 16, tag);
    STAMPT(27);
    BARRIER();
    STAMPT(4);
  }

  v4u wf2[28];
  v4u b1a, b1b;
  unsigned pf0 = 0, pf1 = 0, pf2 = 0, pf3 = 0;
  const int kh = w >> 2, nq = w & 3;
  const unsigned w1vo = OFF_W1 + (d_nt * 8 + w) * 16 * 1024 + lane * 16;
  auto w1load = [&](int i) {
    const unsigned vo = w1vo + (((i >> 2) + nq) & 3) * 4096;
    if ((i & 3) == 0) GLD(wf1[i], vo, p.ws, 0);
    if ((i & 3) == 1) GLD(wf1[i], vo, p.ws, 1024);
    if ((i & 3) == 2) GLD(wf1[i], vo, p.ws, 2048);
    if ((i & 3) == 3) GLD(wf1[i], vo, p.ws, 3072);
  };
  {
    const int xch = tid >> 8, xc = tid & 255;
    const unsigned xoff = OFF_XR + ((2 * cj + xch) * S + 8 * xc) * 2;
    const int c = i16, al = c & 1;
    const int sa = 256 * q4 + 8 * g + 32 * (c >> 1) + 4 * al;
    v4u x1; v2u xt0, xt1; unsigned nx;
    asm volatile("global_load_dwordx4 %0, %1, off sc1" : "=v"(x1) : "v"((const GAS char*)p.ws + xoff) : "memory");
    asm volatile("global_load_ushort %0, %1, off offset:16 sc1" : "=v"(nx) : "v"((const GAS char*)p.ws + xoff) : "memory");
    asm volatile("global_load_dwordx2 %0, %1, off sc1" : "=v"(xt0) : "v"((const GAS char*)p.ws + OFF_XT + (bch * S + sa) * 2) : "memory");
    asm volatile("global_load_dwordx2 %0, %1, off offset:2048 sc1" : "=v"(xt1) : "v"((const GAS char*)p.ws + OFF_XT + (bch * S + sa) * 2) : "memory");
    dma2s((const GAS char*)p.ws + OFF_ST + w * 2048 + lane * 16, LB + B_ST + w * 2048);
    WAIT_V(4);
    asm volatile("" : "+v"(x1), "+v"(nx));
    const float dch = bflo(dv);
    {
      if (lane < 32) *(LAS u16*)(L + B_HC + w * B_HCS + 2 * lane) = ((lane & 31) + 2 * q4 - 30) >= 0 ? (u16)hval : (u16)0;
      nx = xc < 255 ? nx : 0u;
      const v4u x0 = {__builtin_amdgcn_alignbit(x1.y, x1.x, 16), __builtin_amdgcn_alignbit(x1.z, x1.y, 16),
                      __builtin_amdgcn_alignbit(x1.w, x1.z, 16), __builtin_amdgcn_alignbit(nx, x1.w, 16)};
      LAS unsigned char* xb = L + B_XC + xch * 2 * B_XCS + 16 * xc;
      *(LAS v4u*)xb = x0;
      *(LAS v4u*)(xb + B_XCS) = x1;
    }
    BARRIER();
    STAMPT(5);

    LAS unsigned char* ab = L + B_HC + (chl * 4 + (c & 3)) * B_HCS + 2 * (224 + 8 * (c >> 2) + 8 * g) - 128 * q4;
    LAS unsigned char* bb = L + B_XC + (chl * 2 + al) * B_XCS + 2 * (2240 - 1792 - 32 * (c >> 1) + 8 * g) - 128 * q4;
    LAS f32x4* red = (LAS f32x4*)(L + B_RED);
    const int rw = w;
    auto bfinish = [&](int hf, v2u xt) {
      LAS f32x4* rp = red + (chl * 32 + hf * 4 + q4) * 64 + lane;
      const f32x4 a0 = rp[0], a1 = rp[8 * 64], a2 = rp[16 * 64], a3 = rp[24 * 64];
      const int s = sa + 1024 * hf;
      const v2u kvr = *(LAS v2u*)(L + B_XC + (chl * 2 + 1) * B_XCS + 2 * (S - 4 - s));
      v4u g0 = *(LAS v4u*)(L + B_ST + 8 * s), g1 = *(LAS v4u*)(L + B_ST + 8 * s + 16);
      if (__builtin_amdgcn_ballot_w64(g0.y == tag && g0.w == tag && g1.y == tag && g1.w == tag) != ~0ull) {
        for (int spins = 0; spins < SPIN_CAP; ++spins) {
          unsigned long long e[4];
#pragma unroll
          for (int i = 0; i < 4; ++i) e[i] = __hip_atomic_load((const GAS unsigned long long*)(p.ws + OFF_ST + 8 * s) + i, __ATOMIC_RELAXED, __HIP_MEMORY_SCOPE_AGENT);
          g0 = v4u{(unsigned)e[0], (unsigned)(e[0] >> 32), (unsigned)e[1], (unsigned)(e[1] >> 32)};
          g1 = v4u{(unsigned)e[2], (unsigned)(e[2] >> 32), (unsigned)e[3], (unsigned)(e[3] >> 32)};
          if (__builtin_amdgcn_ballot_w64(g0.y == tag && g0.w == tag && g1.y == tag && g1.w == tag) == ~0ull) break;
          __builtin_amdgcn_s_sleep(2);
        }
      }
      const unsigned u0 = g0.x, u1 = g0.z, u2 = g1.x, u3 = g1.z;
      const f32x2 mr01 = {bflo(u0), bflo(u1)}, rr01 = {bfhi(u0), bfhi(u1)}, mr23 = {bflo(u2), bflo(u3)}, rr23 = {bfhi(u2), bfhi(u3)};
      const f32x2 ylo = (a0.xy + a1.xy) + (a2.xy + a3.xy), yhi = (a0.zw + a1.zw) + (a2.zw + a3.zw);
      const float s0_ = al ? ylo.x : yhi.x, s1_ = al ? ylo.y : yhi.y;
      const float r0_ = asf(__builtin_amdgcn_update_dpp(0, asu(s0_), 0xB1, 0xf, 0xf, true));
      const float r1_ = asf(__builtin_amdgcn_update_dpp(0, asu(s1_), 0xB1, 0xf, 0xf, true));
      const f32x2 y01 = {al ? r0_ : ylo.x, al ? yhi.x : r0_}, y23 = {al ? r1_ : ylo.y, al ? yhi.y : r1_};
      const unsigned k1 = kvr.y, k0 = kvr.x;
      const f32x2 x01 = up2(xt.x), x23 = up2(xt.y);
      const f32x2 dd = {dch, dch};
      const f32x2 t01 = fma2(f32x2{bfhi(k1), bflo(k1)}, dd, y01), t23 = fma2(f32x2{bfhi(k0), bflo(k0)}, dd, y23);
      const f32x2 h01 = fma2(fma2(x01, rr01, mr01), t01, x01), h23 = fma2(fma2(x23, rr23, mr23), t23, x23);
      const int sl = (s & 63) >> 2;
      __builtin_amdgcn_raw_buffer_store_b64(v2u{pk(h01.x, h01.y), pk(h23.x, h23.y)}, rs,
                                            OFF_HT + ((s >> 6) * D + bch) * 128 + ((sl ^ (((bch >> 1) & 1) << 3)) << 3), 0, SC1);
    };
    {
      v4u af[8], bf[8], ag[8], bg[8];
      f32x4 acc[4];
#pragma unroll
      for (int i = 0; i < 4; ++i) acc[i] = f32x4{0.f, 0.f, 0.f, 0.f};
#pragma unroll
      for (int i = 0; i < 4; ++i) { bf[i] = *(LAS v4u*)(bb + 512 * (7 - i)); af[i] = *(LAS v4u*)(ab + 512 * i); }
#pragma unroll
      for (int i = 0; i < 4; ++i) { bg[i] = *(LAS v4u*)(bb - 64 + 512 * (7 - i)); ag[i] = *(LAS v4u*)(ab - 64 + 512 * i); }
      SCHED();
#pragma unroll
      for (int ip = 0; ip < 4; ++ip)
#pragma unroll
        for (int i = ip; i < 4; ++i) acc[i] = mfma16(af[i - ip], bf[ip], acc[i]);
#pragma unroll
      for (int i = 4; i < 8; ++i) { bf[i] = *(LAS v4u*)(bb + 512 * (7 - i)); af[i] = *(LAS v4u*)(ab + 512 * i); }
      SCHED();

#pragma unroll
      for (int i = 0; i < 4; ++i)
#pragma unroll
        for (int ip = 0; ip <= i; ++ip) acc[i] = mfma16(ag[i - ip], bg[ip], acc[i]);
      SCHED();
      asm volatile("s_waitcnt vmcnt(0)" : "+v"(xt0), "+v"(xt1) :: "memory");
      if (w >= 4 && bid < 224) {
#pragma unroll
        for (int cc = 0; cc < 2; ++cc) {
          const int v = w & 3;
          const int off = v * 2048 + (16 * cc + 8 * hh + tq) * 64 + 32 * blk + 16 * (tp & 1) + 8 * (tp >> 1);
          const v4u f1 = trfrag(L + A_W1 + off, 256);
          const v4u f2 = trfrag(L + A_W2 + off, 256);
          const int k16 = 2 * (bid / 14) + cc, nt = bid % 14;
          st16(f1, rs, OFF_W1 + (((nt * 8 + (k16 >> 4) * 4 + v) * 16) + (k16 & 15)) * 1024 + lane * 16);
          const int ka = 2 * (bid >> 2) + cc, dt = 2 * (bid & 3) + (v >> 1);
          st16(f2, rs, OFF_W2 + (((dt * 8 + ka / 14) * 28) + (ka % 14) * 2 + (v & 1)) * 1024 + lane * 16);
        }
      }
#pragma unroll
      for (int i = 0; i < 4; ++i) red[(rw * 8 + i) * 64 + lane] = acc[i];
      BARRIER();
      STAMPT(21);
      f32x4 ac2[4];
#pragma unroll
      for (int i = 0; i < 4; ++i) ac2[i] = f32x4{0.f, 0.f, 0.f, 0.f};
      bfinish(0, xt0);
      STAMPT(26);
      SCHED();
#pragma unroll
      for (int ip = 0; ip < 8; ++ip) {
#pragma unroll
        for (int i = (ip > 4 ? ip : 4); i < 8; ++i) ac2[i - 4] = mfma16(af[i - ip], bf[ip], ac2[i - 4]);
        SCHED();
        if (ip < 4) { ag[4 + ip] = *(LAS v4u*)(ab - 64 + 512 * (4 + ip)); bg[4 + ip] = *(LAS v4u*)(bb - 64 + 512 * (7 - 4 - ip)); }
        SCHED();
      }
      WAIT_V(0);
      lds_count_flag8(L, 4, lane, tag, rs, OFF_FB0 + bid * 16);
      asm volatile("global_load_dwordx4 %0, %1, %2" : "=v"(b1a) : "v"((unsigned)(128 * d_nt + 32 * nq + 8 * hh) * 2), "s"(p.b1) : "memory");
      asm volatile("global_load_dwordx4 %0, %1, %2 offset:32" : "=v"(b1b) : "v"((unsigned)(128 * d_nt + 32 * nq + 8 * hh) * 2), "s"(p.b1) : "memory");
      SCHED();
#pragma unroll
      for (int i = 4; i < 8; ++i) {
#pragma unroll
        for (int ip = 0; ip <= i; ++ip) {
          ac2[i - 4] = mfma16(ag[i - ip], bg[ip], ac2[i - 4]);
        }
        SCHED();
        if (i > 4) red[(rw * 8 + 4 + i - 5) * 64 + lane] = ac2[i - 5];
        SCHED();
      }
      red[(rw * 8 + 7) * 64 + lane] = ac2[3];
    }
    BARRIER();
    STAMPT(6);
    if (w == 0) LOOK_ISSUE(OFF_FB0 + xcd * 4096 + lane * 16);
    bfinish(1, xt1);
    STAMPT(7);
    if (w == 0) LOOK_CHECK(1, OFF_FB0 + xcd * 4096 + lane * 16);
    STAMPT(8);
  }

  {
    BARRIER();
    STAMPT(9);
    const GAS unsigned char* tsrc = (const GAS unsigned char*)p.ws + OFF_HT + (size_t)d_mt * 65536 + w * 8192 + lane * 16;
    const unsigned w2off = OFF_W2 + (e_dt * 8 + w) * 28 * 1024 + lane * 16;
    auto second_wait = [&]() {
      if (w == 0) { poll4(rs, OFF_FB1 + xcd * 4096 + lane * 16, tag); lds_set(L, 0); }
      lds_wait(L, 0);
    };
    auto second_res = [&]() {
      const int rr = 16 * (w & 3) + (lane >> 2);
      const int dch2 = 64 * e_dt + rr;
      dma1s((const GAS unsigned char*)p.ws + OFF_HT + ((16 * (w >> 2) + e_mt) * D + dch2) * 128 + (((unsigned)e_j ^ ((dch2 >> 1) & 1)) << 6) + 16 * (lane & 3),
            LB + E_RES + w * 1024);
    };
    if (d_act) { dma4s(tsrc, LB + D_TILE + w * 8192); dma4s(tsrc + 4096, LB + D_TILE + w * 8192 + 4096); }
    if (d_act) { WAIT_V(8); } else { WAIT_V(0); }
    lds_count_flag8(L, 5, lane, tag, rs, OFF_FB1 + bid * 16);
    if (d_act) {
      asm volatile("" : "+v"(b1a), "+v"(b1b) :: "memory");
#pragma unroll
      for (int i = 0; i < 8; ++i) w1load(i);
      SCHED();
      const unsigned basel = (unsigned)(D_TILE + (256 * kh + 8 * hh + tq) * 128 + 32 * blk + 8 * tp);
      const unsigned fsw = ((tq >> 1) & 1) << 6;
      const unsigned adr[2] = {basel + ((unsigned)(kh << 6) ^ fsw), basel + ((unsigned)((kh ^ 1) << 6) ^ fsw)};
      f32x16 acc[2][2];
      auto fraddr = [&](int c, int m) { return L + (adr[m & 1] + (unsigned)(c * 65536 + 8192 * ((((m >> 1) >> 2) + nq) & 3)) + 2048 * ((m >> 1) & 3)); };
      float xg[16];
      unsigned og[8];
      auto hstore = [&](int c) {
        const unsigned ho = OFF_HF + ((32 * c + 2 * d_mt + kh) * 112 + 8 * d_nt + 2 * nq) * 1024 + lane * 16;
        st16(v4u{og[0], og[1], og[2], og[3]}, rs, ho);
        st16(v4u{og[4], og[5], og[6], og[7]}, rs, ho + 1024);
      };
      float ge[16];
      auto gelu_1 = [&](int e) { const float x = xg[e]; ge[e] = __builtin_amdgcn_exp2f(x * __builtin_fmaf(x * x, -0.10294324f, -2.3022082f)); };
      auto gelu_2 = [&](int e) { ge[e] = __builtin_amdgcn_rcpf(ge[e] + 1.0f); };
      auto gelu_3 = [&](int e) {
        float y = xg[e] * ge[e];
        asm("" : "+v"(y));
        xg[e] = y;
        if (e & 1) og[e >> 1] = pk(xg[e - 1], xg[e]);
      };
      float cs0 = 0.f, cs1 = 0.f;
      auto mstep = [&](int c) {
        float s1[2][2] = {{0.f, 0.f}, {0.f, 0.f}}, s2[2][2] = {{0.f, 0.f}, {0.f, 0.f}};
#pragma unroll
        for (int r = 0; r < 2; ++r)
#pragma unroll
          for (int e = 0; e < 16; ++e) acc[c][r][e] = 0.f;
        v4u fr[4];
#pragma unroll
        for (int n = 0; n < 3; ++n) fr[n] = trfrag(fraddr(c, n), 512);
        SCHED();
#pragma unroll
        for (int n = 0; n < 32; ++n) {
          const int i = n >> 1, r = n & 1;
          if (c == 0 && r == 0) {
            if (n < 16) { w1load(8 + i); WAITF(8, wf1[i]); }
            if (n == 16) {
              WAITF(7, wf1[8]);
              if (w == 0) { LOOK_CHECK(5, OFF_FB1 + xcd * 4096 + lane * 16); lds_set(L, 0); }
              lds_wait(L, 0);
            }
            if (n == 18) WAITF(8, wf1[9]);
            if (n == 20) WAITF(9, wf1[10]);
            if (n == 22) WAITF(10, wf1[11]);
            if (n == 24) WAITF(11, wf1[12]);
            if (n == 26) WAITF(11, wf1[13]);
            if (n == 28) WAITF(11, wf1[14]);
            if (n == 30) WAITF(11, wf1[15]);
          }
          if (c == 0 && n == 5 && w == 0) LOOK_ISSUE(OFF_FB1 + xcd * 4096 + lane * 16);
          if (c == 0 && n >= 16 && n < 24) dma1s(tsrc + 16 * 65536 + (n - 16) * 1024, LB + D_TILE + 65536 + w * 8192 + (n - 16) * 1024);
          if (c == 0 && n == 24) second_res();
          if (c == 0 && n >= 16) {
            const v4u wv = wf1[n - 16];
            cs0 = dot2(wv.x, ONES, cs0); cs1 = dot2(wv.y, ONES, cs1); cs0 = dot2(wv.z, ONES, cs0); cs1 = dot2(wv.w, ONES, cs1);
          }
          if (n + 3 < 32) fr[(n + 3) & 3] = trfrag(fraddr(c, n + 3), 512);
          acc[c][r] = mfma32(wf1[i], fr[n & 3], acc[c][r]);
          if (i < 4) {
            const v4u hf = fr[n & 3];
            s1[r][0] = dot2(hf.x, ONES, s1[r][0]); s2[r][0] = dot2(hf.x, hf.x, s2[r][0]);
            s1[r][1] = dot2(hf.y, ONES, s1[r][1]); s2[r][1] = dot2(hf.y, hf.y, s2[r][1]);
            s1[r][0] = dot2(hf.z, ONES, s1[r][0]); s2[r][0] = dot2(hf.z, hf.z, s2[r][0]);
            s1[r][1] = dot2(hf.w, ONES, s1[r][1]); s2[r][1] = dot2(hf.w, hf.w, s2[r][1]);
          }
          if (c == 1) {
            if (n >= 4 && n < 20) gelu_1(n - 4);
            if (n >= 6 && n < 22) gelu_2(n - 6);
            if (n >= 8 && n < 24) gelu_3(n - 8);
            if (n == 25) hstore(0);
          }
          if (c == 0 && (n & 1) == 0 && n >= 26) wf2[(n - 26) >> 1] = ld16(rs, w2off + ((n - 26) >> 1) * 1024);
          if (c == 1 && (n & 1) == 0) wf2[3 + (n >> 1)] = ld16(rs, w2off + (3 + (n >> 1)) * 1024);
          SCHED();
        }
#pragma unroll
        for (int r = 0; r < 2; ++r) {
          const float a = halfsum(s1[r][0] + s1[r][1]), b = halfsum(s2[r][0] + s2[r][1]);
          if (lane < 32) *(LAS f32x2*)(L + D_STAT + (((c * 8 + w) * 2 + ((r + kh) & 1)) * 32 + lane) * 8) = f32x2{a, b};
        }
        if (c == 0) {
          const float t = halfsum(cs0 + cs1);
          if (lane < 32) *(LAS float*)(L + D_CS + (w * 32 + lane) * 4) = t;
        }
      };
      auto xwrite = [&](int c) {
        LAS f32x4* ex = (LAS f32x4*)(L + D_TILE + c * 32768) + w * 256 + lane;
#pragma unroll
        for (int qd = 0; qd < 4; ++qd) ex[qd * 64] = f32x4{acc[c][1][4 * qd], acc[c][1][4 * qd + 1], acc[c][1][4 * qd + 2], acc[c][1][4 * qd + 3]};
      };
      auto xpre = [&](int c) {
        LAS f32x4* ex = (LAS f32x4*)(L + D_TILE + c * 32768) + (w ^ 4) * 256 + lane;
        f32x2 sa = {0.f, 0.f};
#pragma unroll
        for (int ww = 0; ww < 8; ++ww) sa += *(LAS f32x2*)(L + D_STAT + (((c * 8 + ww) * 2 + kh) * 32 + (lane & 31)) * 8);
        const float mean = sa.x * (1.0f / D);
        float var = __builtin_fmaf(-mean, mean, sa.y * (1.0f / D));
        var = var > 0.f ? var : 0.f;
        const float r1 = __builtin_amdgcn_rsqf(var + 1e-5f);
        const float rstd = r1 * __builtin_amdgcn_rsqf(__builtin_fmaf(var * r1, r1, 1e-5f));
        const float nm = -rstd * mean;
        const f32x2 rs2 = {rstd, rstd}, nm2 = {nm, nm};
        LAS f32x4* c4 = (LAS f32x4*)(L + D_CS) + nq * 8 + hh;
        const unsigned bb[8] = {b1a.x, b1a.y, b1a.z, b1a.w, b1b.x, b1b.y, b1b.z, b1b.w};
#pragma unroll
        for (int qd = 0; qd < 4; ++qd) {
          const f32x4 pp = ex[qd * 64];
          const f32x4 cs = c4[2 * qd] + c4[32 + 2 * qd];
#pragma unroll
          for (int hf = 0; hf < 2; ++hf) {
            const int e = 2 * qd + hf;
            const f32x2 a = f32x2{acc[c][0][2 * e], acc[c][0][2 * e + 1]} + (hf ? pp.zw : pp.xy);
            const f32x2 x = fma2(rs2, a, fma2(nm2, hf ? cs.zw : cs.xy, up2(bb[e])));
            xg[2 * e] = x.x; xg[2 * e + 1] = x.y;
            if (c == 0) { asm("" : "+v"(xg[2 * e])); asm("" : "+v"(xg[2 * e + 1])); }
          }
        }
      };
      auto gelu_all = [&]() {
#pragma unroll
        for (int e = 0; e < 8; ++e) {
          const f32x2 x = {xg[2 * e], xg[2 * e + 1]};
          const f32x2 pz = fma2(x * x, f32x2{-0.10294324f, -0.10294324f}, f32x2{-2.3022082f, -2.3022082f});
          const f32x2 z = x * pz;
          const f32x2 den = f32x2{__builtin_amdgcn_exp2f(z.x), __builtin_amdgcn_exp2f(z.y)} + f32x2{1.f, 1.f};
          const f32x2 y = x * f32x2{__builtin_amdgcn_rcpf(den.x), __builtin_amdgcn_rcpf(den.y)};
          og[e] = pk(y.x, y.y);
        }
      };
      WAIT_V(8);
      BARRIER();
      STAMPT(10);
      SCHED();
      mstep(0);
      STAMPT(22);
      WAIT_V(3);
      BARRIER();
      STAMPT(11);
      xwrite(0);
      BARRIER();
      STAMPT(23);
      xpre(0);
      STAMPT(24);
      mstep(1);
      STAMPT(25);
      xwrite(1);
      BARRIER();
      STAMPT(12);
      xpre(1);
      WAIT_V(3);
      lds_count_flag(L, 2, 8, tid, d_act, tag, rs, OFF_FD + d_mt * 256 + d_nt * 16);
      gelu_all();
      hstore(1);
#pragma unroll
      for (int i = 19; i < 28; ++i) wf2[i] = ld16(rs, w2off + i * 1024);
      SCHED();
      STAMPT(13);
    } else {
      second_wait();
      second_res();
#pragma unroll
      for (int i = 0; i < 28; ++i) wf2[i] = ld16(rs, w2off + i * 1024);
    }
  }

  {
    const int sbo = w >> 2, dbo = (w >> 1) & 1, hf = w & 1;
    const v4u b2v = *(const GAS v4u*)((const GAS char*)p.b2 + (unsigned)(64 * e_dt + 32 * dbo + 16 * hf + 8 * hh) * 2);
    if (w == 0) poll1(rs, OFF_FD + e_mt * 256 + (lane < 14 ? lane : 0) * 16, tag);
    WAIT_V(10);
    BARRIER();
    if (d_act && tid == 0) st4(tag, rs, OFF_FD + 4096 + d_mt * 256 + d_nt * 16);
    STAMPT(14);
    const unsigned hoff0 = OFF_HF + ((2 * e_mt + e_j) * 112 + 14 * w) * 1024 + lane * 16, hoff1 = hoff0 + 32 * 112 * 1024;
    if (w == 0) { poll1(rs, OFF_FD + 4096 + e_mt * 256 + (lane < 14 ? lane : 0) * 16, tag); lds_set(L, 1); }
    v4u hf0[14], hf1[14];
#pragma unroll
    for (int k = 0; k < 14; ++k) hf0[k] = ld16(rs, hoff0 + k * 1024);
    SCHED();
    lds_wait(L, 1);
    STAMPT(15);
    LAS f32x4* red = (LAS f32x4*)(L + E_RED + w * 16384) + lane;
    {
      f32x16 acc[2];
#pragma unroll
      for (int e = 0; e < 16; ++e) { acc[0][e] = 0.f; acc[1][e] = 0.f; }
#pragma unroll
      for (int k = 0; k < 14; ++k) {
        acc[0] = mfma32(wf2[2 * k], hf0[k], acc[0]); acc[1] = mfma32(wf2[2 * k + 1], hf0[k], acc[1]);
        SCHED();
        hf1[k] = ld16(rs, hoff1 + k * 1024);
        SCHED();
      }
#pragma unroll
      for (int d = 0; d < 2; ++d)
#pragma unroll
        for (int qd = 0; qd < 4; ++qd) red[(d * 4 + qd) * 64] = f32x4{acc[d][4 * qd], acc[d][4 * qd + 1], acc[d][4 * qd + 2], acc[d][4 * qd + 3]};
    }
    STAMPT(16);
    {
      f32x16 acc[2];
#pragma unroll
      for (int e = 0; e < 16; ++e) { acc[0][e] = 0.f; acc[1][e] = 0.f; }
#pragma unroll
      for (int k = 0; k < 14; ++k) { acc[0] = mfma32(wf2[2 * k], hf1[k], acc[0]); acc[1] = mfma32(wf2[2 * k + 1], hf1[k], acc[1]); }
#pragma unroll
      for (int d = 0; d < 2; ++d)
#pragma unroll
        for (int qd = 0; qd < 4; ++qd) red[512 + (d * 4 + qd) * 64] = f32x4{acc[d][4 * qd], acc[d][4 * qd + 1], acc[d][4 * qd + 2], acc[d][4 * qd + 3]};
    }
    BARRIER();
    STAMPT(17);
    f32x2 o[4] = {{0.f, 0.f}, {0.f, 0.f}, {0.f, 0.f}, {0.f, 0.f}};
#pragma unroll
    for (int ww = 0; ww < 8; ++ww) {
      LAS f32x4* rp = (LAS f32x4*)(L + E_RED + ww * 16384) + sbo * 512 + (dbo * 4 + 2 * hf) * 64 + lane;
      const f32x4 a = rp[0], b = rp[64];
      o[0] += a.xy; o[1] += a.zw; o[2] += b.xy; o[3] += b.zw;
    }
    const v4u hr = trfrag(L + E_RES + sbo * 4096 + (32 * dbo + 16 * hf + 8 * hh + tq) * 64 + 32 * blk + 8 * tp, 256);
    o[0] += up2(b2v.x) + up2(hr.x); o[1] += up2(b2v.y) + up2(hr.y);
    o[2] += up2(b2v.z) + up2(hr.z); o[3] += up2(b2v.w) + up2(hr.w);
    const v4u ov = {pk(o[0].x, o[0].y), pk(o[1].x, o[1].y), pk(o[2].x, o[2].y), pk(o[3].x, o[3].y)};
    {
      const rsrc_t ro = __builtin_amdgcn_make_buffer_rsrc((void*)p.out, 0, S * D * 2, 0x00020000);
      st16(ov, ro, (unsigned)((1024 * sbo + 64 * e_mt + 32 * e_j + (lane & 31)) * D + 64 * e_dt + 32 * dbo + 16 * hf + 8 * hh) * 2);
    }
    if (bid == 0 && tid == 0) st4(tag, rs, OFF_EP);
    STAMPT(18);
  }
}

extern "C" void kernel_launch(void* const* d_in, const int* in_sizes, int n_in, void* d_out, int out_size, void* d_ws,
                              size_t ws_size, hipStream_t stream) {
  Params p;
  p.q = (const u16*)d_in[0]; p.k = (const u16*)d_in[1]; p.v = (const u16*)d_in[2];
  p.shk = (const u16*)d_in[3]; p.ssk = (const u16*)d_in[4]; p.ssd = (const u16*)d_in[5];
  p.w1 = (const u16*)d_in[7]; p.b1 = (const u16*)d_in[8]; p.w2 = (const u16*)d_in[9]; p.b2 = (const u16*)d_in[10];
  p.out = (u16*)d_out; p.ws = (unsigned char*)d_ws;
  fused<<<256, 512, 0, stream>>>(p);
}
```

```cpp
#include <hip/hip_runtime.h>
#include <hip/hip_bf16.h>

#define GAS __attribute__((address_space(1)))
#define LAS __attribute__((address_space(3)))
typedef unsigned short u16;
typedef unsigned v4u __attribute__((ext_vector_type(4)));
typedef unsigned v2u __attribute__((ext_vector_type(2)));
typedef float f32x16 __attribute__((ext_vector_type(16)));
typedef float f32x4 __attribute__((ext_vector_type(4)));
typedef float f32x2 __attribute__((ext_vector_type(2)));
typedef short s16x4 __attribute__((ext_vector_type(4)));
typedef short bf16x8 __attribute__((ext_vector_type(8)));
typedef __bf16 bf2 __attribute__((ext_vector_type(2)));
typedef __amdgpu_buffer_rsrc_t rsrc_t;

constexpr int S = 2048, D = 512, F = 1792;
constexpr unsigned OFF_EP = 0;
constexpr unsigned OFF_FA1 = 4096;
constexpr unsigned OFF_FD = 8192;
constexpr unsigned OFF_FB0 = 16384;
constexpr unsigned OFF_FB1 = OFF_FB0 + 32768;
constexpr unsigned OFF_XR = 262144;
constexpr unsigned OFF_XT = OFF_XR + D * S * 2;
constexpr unsigned OFF_HT = OFF_XT + D * S * 2;
constexpr unsigned OFF_ST = OFF_HT + D * S * 2;
constexpr unsigned OFF_W1 = OFF_ST + 16384;
constexpr unsigned OFF_W2 = OFF_W1 + D * F * 2;
constexpr unsigned OFF_HF = OFF_W2 + D * F * 2;
constexpr unsigned WS_END = OFF_HF + S * F * 2;
constexpr int SPIN_CAP = 1 << 16;

struct Params {
  const u16 *q, *k, *v, *shk, *ssk, *ssd, *w1, *b1, *w2, *b2;
  u16* out;
  unsigned char* ws;
};

__device__ __forceinline__ float asf(unsigned u) { return __builtin_bit_cast(float, u); }
__device__ __forceinline__ unsigned asu(float f) { return __builtin_bit_cast(unsigned, f); }
__device__ __forceinline__ float bflo(unsigned u) { return asf(u << 16); }
__device__ __forceinline__ float bfhi(unsigned u) { return asf(u & 0xffff0000u); }
__device__ __forceinline__ unsigned pk(float lo, float hi) {
  f32x2 f = {lo, hi};
  return __builtin_bit_cast(unsigned, __builtin_convertvector(f, bf2));
}
__device__ __forceinline__ f32x2 up2(unsigned u) { return f32x2{bflo(u), bfhi(u)}; }
__device__ __forceinline__ f32x2 fma2(f32x2 a, f32x2 b, f32x2 c) { return __builtin_elementwise_fma(a, b, c); }
__device__ __forceinline__ f32x4 mfma16(v4u a, v4u b, f32x4 c) {
  return __builtin_amdgcn_mfma_f32_16x16x32_bf16(__builtin_bit_cast(bf16x8, a), __builtin_bit_cast(bf16x8, b), c, 0, 0, 0);
}
__device__ __forceinline__ f32x16 mfma32(v4u a, v4u b, f32x16 c) {
  return __builtin_amdgcn_mfma_f32_32x32x16_bf16(__builtin_bit_cast(bf16x8, a), __builtin_bit_cast(bf16x8, b), c, 0, 0, 0);
}
__device__ __forceinline__ v2u trread(LAS unsigned char* p) {
  return __builtin_bit_cast(v2u, __builtin_amdgcn_ds_read_tr16_b64_v4i16((LAS s16x4*)p));
}
__device__ __forceinline__ v4u trfrag(LAS unsigned char* p, int second) {
  const v2u a = trread(p), b = trread(p + second);
  return v4u{a.x, a.y, b.x, b.y};
}
__device__ __forceinline__ float dot2(unsigned a, unsigned b, float c) {
  return __builtin_amdgcn_fdot2_f32_bf16(__builtin_bit_cast(bf2, a), __builtin_bit_cast(bf2, b), c, false);
}
constexpr int SC1 = 16;
#define DMA_HEAD "s_mov_b32 m0, %1\n\ts_nop 0\n\t"
#define DMA_P(o, f) "global_load_lds_dwordx4 %0, off offset:" #o f "\n\t"
__device__ __forceinline__ void dma4(const GAS void* g, unsigned lds) {
  asm volatile(DMA_HEAD DMA_P(0, "") DMA_P(1024, "") DMA_P(2048, "") DMA_P(3072, "") :: "v"(g), "s"(lds) : "memory");
}
__device__ __forceinline__ void dma3(const GAS void* g, unsigned lds) {
  asm volatile(DMA_HEAD DMA_P(0, "") DMA_P(1024, "") DMA_P(2048, "") :: "v"(g), "s"(lds) : "memory");
}
__device__ __forceinline__ void dma1(const GAS void* g, unsigned lds) {
  asm volatile(DMA_HEAD DMA_P(0, "") :: "v"(g), "s"(lds) : "memory");
}
__device__ __forceinline__ void dma4s(const GAS void* g, unsigned lds) {
  asm volatile(DMA_HEAD DMA_P(0, " sc1") DMA_P(1024, " sc1") DMA_P(2048, " sc1") DMA_P(3072, " sc1") :: "v"(g), "s"(lds) : "memory");
}
__device__ __forceinline__ void dma3s(const GAS void* g, unsigned lds) {
  asm volatile(DMA_HEAD DMA_P(0, " sc1") DMA_P(1024, " sc1") DMA_P(2048, " sc1") :: "v"(g), "s"(lds) : "memory");
}
__device__ __forceinline__ void dma2s(const GAS void* g, unsigned lds) {
  asm volatile(DMA_HEAD DMA_P(0, " sc1") DMA_P(1024, " sc1") :: "v"(g), "s"(lds) : "memory");
}
__device__ __forceinline__ void dma1s(const GAS void* g, unsigned lds) {
  asm volatile(DMA_HEAD DMA_P(0, " sc1") :: "v"(g), "s"(lds) : "memory");
}
__device__ __forceinline__ v4u ld16(rsrc_t rs, unsigned off) { return __builtin_amdgcn_raw_buffer_load_b128(rs, off, 0, SC1); }
__device__ __forceinline__ void st16(v4u v, rsrc_t rs, unsigned off) { __builtin_amdgcn_raw_buffer_store_b128(v, rs, off, 0, SC1); }
__device__ __forceinline__ void st4(unsigned v, rsrc_t rs, unsigned off) { __builtin_amdgcn_raw_buffer_store_b32(v, rs, off, 0, SC1); }
template <int CTRL, int RM>
__device__ __forceinline__ float dppadd(float x) {
  return x + asf(__builtin_amdgcn_update_dpp(0, asu(x), CTRL, RM, 0xf, true));
}
__device__ __forceinline__ float wavesum(float x) {
  x = dppadd<0x111, 0xf>(x); x = dppadd<0x112, 0xf>(x); x = dppadd<0x114, 0xf>(x); x = dppadd<0x118, 0xf>(x);
  x = dppadd<0x142, 0xa>(x); x = dppadd<0x143, 0xc>(x);
  return x;
}
__device__ __forceinline__ float halfsum(float x) {
  const unsigned u = asu(x);
  auto r = __builtin_amdgcn_permlane32_swap(u, u, false, false);
  const unsigned r0 = r[0], r1 = r[1];
  return asf(r0) + asf(r1);
}
#define WAIT_V(n) asm volatile("s_waitcnt vmcnt(" #n ")" ::: "memory")
#define BARRIER() do { asm volatile("s_waitcnt lgkmcnt(0)" ::: "memory"); __builtin_amdgcn_s_barrier(); asm volatile("" ::: "memory"); } while (0)
#define SCHED() __builtin_amdgcn_sched_barrier(0)
constexpr unsigned ONES = 0x3f803f80u;

#define STAMPT(idx)

#define SEQ 1
#define CODEPF 0
constexpr unsigned CODE_BYTES = 22016;
constexpr int LDS_BYTES = 163840;
constexpr int A_K = 0, A_V = A_K + 68 * 128, A_Q = A_V + 8192, A_END = A_Q + 8192;
constexpr int B_HCS = 4160, B_XCS = 4640;
constexpr int L_FLAG = LDS_BYTES - 64;
constexpr int B_XC = L_FLAG - 4 * B_XCS, B_HC = B_XC - 8 * B_HCS;
constexpr int B_RED = 0, B_ST = 65536, A_W1 = B_ST + 16384, A_W2 = A_W1 + 8192;
constexpr int D_TILE = 0, D_STAT = 131072, D_CS = D_STAT + 2 * 8 * 64 * 8, D_END = D_CS + 1024;
constexpr int E_RED = 0, E_RES = 143360;
static_assert(A_W2 + 8192 <= B_HC && A_END <= 65536 && B_HC % 16 == 0 && D_END <= E_RES && E_RES + 8192 <= L_FLAG, "");

__device__ __forceinline__ void poll4(rsrc_t rs, unsigned off, unsigned tag) {
  for (int spins = 0; spins < SPIN_CAP; ++spins) {
    const unsigned f0 = __builtin_amdgcn_raw_buffer_load_b32(rs, off, 0, SC1), f1 = __builtin_amdgcn_raw_buffer_load_b32(rs, off + 1024, 0, SC1);
    const unsigned f2 = __builtin_amdgcn_raw_buffer_load_b32(rs, off + 2048, 0, SC1), f3 = __builtin_amdgcn_raw_buffer_load_b32(rs, off + 3072, 0, SC1);
    if (__builtin_amdgcn_ballot_w64(f0 == tag && f1 == tag && f2 == tag && f3 == tag) == ~0ull) break;
    __builtin_amdgcn_s_sleep(1);
    asm volatile("" ::: "memory");
  }
}
__device__ __forceinline__ void poll1(rsrc_t rs, unsigned off, unsigned tag) {
  for (int spins = 0; spins < SPIN_CAP; ++spins) {
    const unsigned f = __builtin_amdgcn_raw_buffer_load_b32(rs, off, 0, SC1);
    if (__builtin_amdgcn_ballot_w64(f == tag) == ~0ull) break;
    __builtin_amdgcn_s_sleep(1);
    asm volatile("" ::: "memory");
  }
}
__device__ __forceinline__ void lds_set(LAS unsigned char* L, int idx) {
  asm volatile("" ::: "memory");
  *(volatile LAS unsigned*)(L + L_FLAG + 4 * idx) = 1u;
}
__device__ __forceinline__ void lds_wait(LAS unsigned char* L, int idx) {
  for (int spins = 0; spins < (SPIN_CAP << 4); ++spins) {
    if (__builtin_amdgcn_readfirstlane(*(volatile LAS unsigned*)(L + L_FLAG + 4 * idx)) != 0) break;
    __builtin_amdgcn_s_sleep(1);
  }
  asm volatile("" ::: "memory");
}
#define GLD(dst, voff, base, imm) asm volatile("global_load_dwordx4 %0, %1, %2 offset:" #imm " sc1" : "=v"(dst) : "v"(voff), "s"(base) : "memory")
__device__ __forceinline__ void lds_count_flag(LAS unsigned char* L, int idx, unsigned n, int tid, bool act, unsigned tag, rsrc_t rs, unsigned off) {
  if ((tid & 63) == 0) {
    const unsigned old = __hip_atomic_fetch_add((LAS unsigned*)(L + L_FLAG + 4 * idx), 1u, __ATOMIC_RELAXED, __HIP_MEMORY_SCOPE_WORKGROUP);
    if (old == n - 1 && act) st4(tag, rs, off);
  }
}
__device__ __forceinline__ bool lds_count_flag8(LAS unsigned char* L, int idx, int lane, unsigned tag, rsrc_t rs, unsigned off) {
  unsigned old = 0;
  if (lane == 0) old = __hip_atomic_fetch_add((LAS unsigned*)(L + L_FLAG + 4 * idx), 1u, __ATOMIC_RELAXED, __HIP_MEMORY_SCOPE_WORKGROUP);
  const bool last = __builtin_amdgcn_readfirstlane(old) == 7;
  if (last && lane < 8) st4(tag, rs, off + lane * 4096);
  return last;
}
__device__ __forceinline__ void poll5(rsrc_t rs, unsigned offa, unsigned off, unsigned tag) {
  for (int spins = 0; spins < SPIN_CAP; ++spins) {
    const unsigned fa = __builtin_amdgcn_raw_buffer_load_b32(rs, offa, 0, SC1);
    const unsigned f0 = __builtin_amdgcn_raw_buffer_load_b32(rs, off, 0, SC1), f1 = __builtin_amdgcn_raw_buffer_load_b32(rs, off + 1024, 0, SC1);
    const unsigned f2 = __builtin_amdgcn_raw_buffer_load_b32(rs, off + 2048, 0, SC1), f3 = __builtin_amdgcn_raw_buffer_load_b32(rs, off + 3072, 0, SC1);
    if (__builtin_amdgcn_ballot_w64(fa == tag && f0 == tag && f1 == tag && f2 == tag && f3 == tag) == ~0ull) break;
    __builtin_amdgcn_s_sleep(1);
    asm volatile("" ::: "memory");
  }
}
#define LOOK_ISSUE(off) do { const GAS char* fp_ = (const GAS char*)p.ws + (off); \
    asm volatile("global_load_dword %0, %1, off sc1" : "=v"(pf0) : "v"(fp_) : "memory"); \
    asm volatile("global_load_dword %0, %1, off offset:1024 sc1" : "=v"(pf1) : "v"(fp_) : "memory"); \
    asm volatile("global_load_dword %0, %1, off offset:2048 sc1" : "=v"(pf2) : "v"(fp_) : "memory"); \
    asm volatile("global_load_dword %0, %1, off offset:3072 sc1" : "=v"(pf3) : "v"(fp_) : "memory"); } while (0)
#define LOOK_CHECK(n, off) do { asm volatile("s_waitcnt vmcnt(" #n ")" : "+v"(pf0), "+v"(pf1), "+v"(pf2), "+v"(pf3) :: "memory"); \
    if (__builtin_amdgcn_ballot_w64(pf0 == tag && pf1 == tag && pf2 == tag && pf3 == tag) != ~0ull) poll4(rs, (off), tag); } while (0)
#define WAITF(n, x) asm volatile("s_waitcnt vmcnt(" #n ")" : "+v"(x) :: "memory")
#define ALOAD(op, dst, ptr) asm volatile(op " %0, %1, off" : "=v"(dst) : "v"(ptr) : "memory")

__global__ void __launch_bounds__(512, 2) fused(Params p) {
  __shared__ __attribute__((aligned(16))) unsigned char smem[LDS_BYTES];
  LAS unsigned char* L = (LAS unsigned char*)smem;
  const unsigned LB = (unsigned)(size_t)L;
  const int tid = threadIdx.x, lane = tid & 63, w = __builtin_amdgcn_readfirstlane(tid >> 6);
  const int bid = blockIdx.x, xcd = bid & 7, bix = bid >> 3;
  const rsrc_t rs = __builtin_amdgcn_make_buffer_rsrc((void*)p.ws, 0, (int)WS_END, 0x00020000);
  const int g = lane >> 4, i16 = lane & 15, tq = i16 >> 2, tp = i16 & 3;
  const int hh = lane >> 5, blk = g & 1;
  STAMPT(0);

  const int cj = xcd * 32 + bix;
  const bool d_act = bix < 28;
  const int d_mt = 4 * (xcd >> 1) + (d_act ? bix / 7 : 0), d_nt = 7 * (xcd & 1) + bix % 7;
  const int e_mt = 2 * xcd + (bix >> 4), e_j = (bix >> 3) & 1, e_dt = bix & 7;
  const int chl = w >> 2, q4 = w & 3, bch = 2 * cj + chl;

  v4u wf1[16];
  unsigned tag;
  unsigned hval, dv;
  {
    const int b = xcd, a = bix;
    const int ch = 64 * b + 16 * (w & 3) + i16, seg = 4 * (w >> 2) + g;
    const int srow = 64 * a + 8 * b + w;
    v2u shw; v4u qs;
    {
      const int rl = lane >> 3, sl = lane & 7;
      const int rhok = 4 + 8 * w + rl, rhov = 8 * w + rl;
      const unsigned fk = ((rhok >> 1) & 1) | (((rhok >> 3) & 1) << 1), fv = ((rhov >> 1) & 1) | (((rhov >> 3) & 1) << 1);
      const unsigned rowoff = ((64 * a + 8 * w + rl) * D + 64 * b) * 2 + 16 * (sl & 1);
      const unsigned uk = rowoff + 32 * ((sl >> 1) ^ fk), uv = rowoff + 32 * ((sl >> 1) ^ fv);
      if (w == 0 && a > 0 && lane < 32)
        dma1((const GAS char*)p.k + (((64 * a - 4 + rl) * D + 64 * b) * 2 + 16 * (sl & 1) + 32 * ((sl >> 1) ^ ((rl >> 1) & 1))), LB + A_K);
      ALOAD("global_load_dwordx4", qs, (const GAS char*)p.q + (unsigned)(srow * D + 8 * lane) * 2);
      asm volatile("global_load_dword %0, %1, off sc1" : "=v"(tag) : "v"((const GAS char*)p.ws + OFF_EP) : "memory");
      ALOAD("global_load_dwordx2", shw, (const GAS char*)p.shk + (unsigned)ch * 8);
      {
        const int hidx = (lane & 31) + 2 * q4 - 30;
        ALOAD("global_load_ushort", hval, (const GAS u16*)(p.ssk + (size_t)bch * S + (hidx < 0 ? 0 : hidx)));
        ALOAD("global_load_ushort", dv, (const GAS u16*)(p.ssd + bch));
      }
      dma1((const GAS char*)p.k + uk, LB + A_K + 512 + w * 1024);
      dma1((const GAS char*)p.v + uv, LB + A_V + w * 1024);
      dma1((const GAS char*)p.q + uv, LB + A_Q + w * 1024);
    }
    if (a == 0 && tid < 32) *(LAS v4u*)(L + A_K + 16 * tid) = v4u{0, 0, 0, 0};
    if (tid < 16) *(LAS unsigned*)(L + L_FLAG + 4 * tid) = 0u;
    if (tid < 128) *(LAS v4u*)(L + B_XC + (tid >> 5) * B_XCS + 4096 + 16 * (tid & 31)) = v4u{0, 0, 0, 0};
    asm volatile("s_waitcnt vmcnt(3)" : "+v"(qs), "+v"(tag), "+v"(shw), "+v"(hval), "+v"(dv) :: "memory");
    tag += 1u;
    {
      float s1 = dot2(qs.x, ONES, dot2(qs.y, ONES, 0.f)) + dot2(qs.z, ONES, dot2(qs.w, ONES, 0.f));
      float s2 = dot2(qs.x, qs.x, dot2(qs.y, qs.y, 0.f)) + dot2(qs.z, qs.z, dot2(qs.w, qs.w, 0.f));
      s1 = wavesum(s1); s2 = wavesum(s2);
      const float mean = s1 * (1.0f / D);
      float var = __builtin_fmaf(-mean, mean, s2 * (1.0f / D));
      var = var > 0.f ? var : 0.f;
      const float rstd = __builtin_amdgcn_rsqf(var + 1e-5f);
      if (lane == 63) __builtin_amdgcn_raw_buffer_store_b64(v2u{pk(-mean * rstd, rstd), tag}, rs, OFF_ST + 8 * srow, 0, SC1);
    }
    WAIT_V(1);
    BARRIER();
    STAMPT(1);
    const int s0 = 64 * a + 8 * seg;
    const int r0 = 8 * seg + tq;
    const unsigned fr0 = (r0 >> 1) & 1, sg = seg & 1;
    const unsigned ua = ((w & 3) ^ (fr0 | (sg << 1))) * 32 + 8 * tp, ub = ((w & 3) ^ (fr0 | ((sg ^ 1) << 1))) * 32 + 8 * tp;
    {
      LAS unsigned char* kb = L + A_K + r0 * 128;
      const v2u k0 = trread(kb + ua), k1 = trread(kb + 512 + ua), k2 = trread(kb + 1024 + ub);
      LAS unsigned char* vb = L + A_V + r0 * 128 + ua;
      const v2u v0 = trread(vb), v1 = trread(vb + 512);
      LAS unsigned char* qb = L + A_Q + r0 * 128 + ua;
      const v2u q0 = trread(qb), q1 = trread(qb + 512);
      const unsigned E[6] = {k0.x, k0.y, k1.x, k1.y, k2.x, k2.y};
      unsigned O[5];
#pragma unroll
      for (int i = 0; i < 5; ++i) O[i] = __builtin_amdgcn_alignbit(E[i + 1], E[i], 16);
      const unsigned shx = shw.x, shy = shw.y;
      const unsigned w32 = __builtin_amdgcn_alignbit(shy, shy, 16);
      const unsigned w10 = __builtin_amdgcn_alignbit(shx, shx, 16);
      float ks[8];
#pragma unroll
      for (int j = 0; j < 8; ++j) {
        const unsigned pa = (j & 1) ? E[(j + 1) >> 1] : O[j >> 1];
        const unsigned pb = (j & 1) ? E[(j + 3) >> 1] : O[(j + 2) >> 1];
        ks[j] = dot2(pb, w10, dot2(pa, w32, 0.f));
      }
      const f32x2 a0 = f32x2{ks[0], ks[1]} * up2(v0.x), a1 = f32x2{ks[2], ks[3]} * up2(v0.y);
      const f32x2 a2 = f32x2{ks[4], ks[5]} * up2(v1.x), a3 = f32x2{ks[6], ks[7]} * up2(v1.y);
      const v4u xr = {pk(a3.y, a3.x), pk(a2.y, a2.x), pk(a1.y, a1.x), pk(a0.y, a0.x)};
      st16(xr, rs, OFF_XR + (ch * S + (S - 8 - s0)) * 2);
      st16(v4u{q0.x, q0.y, q1.x, q1.y}, rs, OFF_XT + (ch * S + s0) * 2);
    }
    STAMPT(2);
    WAIT_V(0);
    BARRIER();
    if (tid == 0) st4(tag, rs, OFF_FA1 + (b * 32 + a) * 16);
    STAMPT(3);
    if (w >= 4) {
      const int w4 = w - 4, wb = bid < 224 ? bid : 0;
#pragma unroll
      for (int cc = 0; cc < 2; ++cc) {
        const int cw = 2 * w4 + cc;
        const u16* hrow = p.ssk + (size_t)(2 * cj + (cw >> 2)) * S;
        const int idx = 8 * (4 + lane) + 2 * (cw & 3) - 30;
        const unsigned lb = LB + B_HC + cw * B_HCS + 64;
        dma3((const GAS void*)(hrow + idx), lb);
        int idx2 = idx + 3 * 512;
        idx2 = idx2 > S - 8 ? S - 8 : idx2;
        dma1((const GAS void*)(hrow + idx2), lb + 3072);
      }
#pragma unroll
      for (int cc = 0; cc < 2; ++cc) {
        const int wr = 16 * cc + (lane >> 2);
        dma1((const GAS char*)p.w1 + (unsigned)((32 * (wb / 14) + wr) * F + 128 * (wb % 14) + 32 * w4 + 8 * (lane & 3)) * 2, LB + A_W1 + w4 * 2048 + cc * 1024);
        dma1((const GAS char*)p.w2 + (unsigned)((32 * (wb >> 2) + wr) * D + 128 * (wb & 3) + 32 * w4 + 8 * (lane & 3)) * 2, LB + A_W2 + w4 * 2048 + cc * 1024);
      }
    }
    if (w == 0) poll1(rs, OFF_FA1 + (xcd * 32 + (lane & 31)) * 16, tag);
    STAMPT(27);
    BARRIER();
    STAMPT(4);
  }

  v4u wf2[28];
  v4u b1a, b1b;
  unsigned pf0 = 0, pf1 = 0, pf2 = 0, pf3 = 0;
  const int kh = w >> 2, nq = w & 3;
  const unsigned w1vo = OFF_W1 + (d_nt * 8 + w) * 16 * 1024 + lane * 16;
  auto w1load = [&](int i) {
    const unsigned vo = w1vo + (((i >> 2) + nq) & 3) * 4096;
    if ((i & 3) == 0) GLD(wf1[i], vo, p.ws, 0);
    if ((i & 3) == 1) GLD(wf1[i], vo, p.ws, 1024);
    if ((i & 3) == 2) GLD(wf1[i], vo, p.ws, 2048);
    if ((i & 3) == 3) GLD(wf1[i], vo, p.ws, 3072);
  };
  {
    const int xch = tid >> 8, xc = tid & 255;
    const unsigned xoff = OFF_XR + ((2 * cj + xch) * S + 8 * xc) * 2;
    const int c = i16, al = c & 1;
    const int sa = 256 * q4 + 8 * g + 32 * (c >> 1) + 4 * al;
    v4u x1; v2u xt0, xt1; unsigned nx;
    asm volatile("global_load_dwordx4 %0, %1, off sc1" : "=v"(x1) : "v"((const GAS char*)p.ws + xoff) : "memory");
    asm volatile("global_load_ushort %0, %1, off offset:16 sc1" : "=v"(nx) : "v"((const GAS char*)p.ws + xoff) : "memory");
    asm volatile("global_load_dwordx2 %0, %1, off sc1" : "=v"(xt0) : "v"((const GAS char*)p.ws + OFF_XT + (bch * S + sa) * 2) : "memory");
    asm volatile("global_load_dwordx2 %0, %1, off offset:2048 sc1" : "=v"(xt1) : "v"((const GAS char*)p.ws + OFF_XT + (bch * S + sa) * 2) : "memory");
    dma2s((const GAS char*)p.ws + OFF_ST + w * 2048 + lane * 16, LB + B_ST + w * 2048);
    WAIT_V(4);
    asm volatile("" : "+v"(x1), "+v"(nx));
    const float dch = bflo(dv);
    {
      if (lane < 32) *(LAS u16*)(L + B_HC + w * B_HCS + 2 * lane) = ((lane & 31) + 2 * q4 - 30) >= 0 ? (u16)hval : (u16)0;
      nx = xc < 255 ? nx : 0u;
      const v4u x0 = {__builtin_amdgcn_alignbit(x1.y, x1.x, 16), __builtin_amdgcn_alignbit(x1.z, x1.y, 16),
                      __builtin_amdgcn_alignbit(x1.w, x1.z, 16), __builtin_amdgcn_alignbit(nx, x1.w, 16)};
      LAS unsigned char* xb = L + B_XC + xch * 2 * B_XCS + 16 * xc;
      *(LAS v4u*)xb = x0;
      *(LAS v4u*)(xb + B_XCS) = x1;
    }
    BARRIER();
    STAMPT(5);

    LAS unsigned char* ab = L + B_HC + (chl * 4 + (c & 3)) * B_HCS + 2 * (224 + 8 * (c >> 2) + 8 * g) - 128 * q4;
    LAS unsigned char* bb = L + B_XC + (chl * 2 + al) * B_XCS + 2 * (2240 - 1792 - 32 * (c >> 1) + 8 * g) - 128 * q4;
    LAS f32x4* red = (LAS f32x4*)(L + B_RED);
    const int rw = w;
    auto bfinish = [&](int hf, v2u xt) {
      LAS f32x4* rp = red + (chl * 32 + hf * 4 + q4) * 64 + lane;
      const f32x4 a0 = rp[0], a1 = rp[8 * 64], a2 = rp[16 * 64], a3 = rp[24 * 64];
      const int s = sa + 1024 * hf;
      const v2u kvr = *(LAS v2u*)(L + B_XC + (chl * 2 + 1) * B_XCS + 2 * (S - 4 - s));
      v4u g0 = *(LAS v4u*)(L + B_ST + 8 * s), g1 = *(LAS v4u*)(L + B_ST + 8 * s + 16);
      if (__builtin_amdgcn_ballot_w64(g0.y == tag && g0.w == tag && g1.y == tag && g1.w == tag) != ~0ull) {
        for (int spins = 0; spins < SPIN_CAP; ++spins) {
          unsigned long long e[4];
#pragma unroll
          for (int i = 0; i < 4; ++i) e[i] = __hip_atomic_load((const GAS unsigned long long*)(p.ws + OFF_ST + 8 * s) + i, __ATOMIC_RELAXED, __HIP_MEMORY_SCOPE_AGENT);
          g0 = v4u{(unsigned)e[0], (unsigned)(e[0] >> 32), (unsigned)e[1], (unsigned)(e[1] >> 32)};
          g1 = v4u{(unsigned)e[2], (unsigned)(e[2] >> 32), (unsigned)e[3], (unsigned)(e[3] >> 32)};
          if (__builtin_amdgcn_ballot_w64(g0.y == tag && g0.w == tag && g1.y == tag && g1.w == tag) == ~0ull) break;
          __builtin_amdgcn_s_sleep(2);
        }
      }
      const unsigned u0 = g0.x, u1 = g0.z, u2 = g1.x, u3 = g1.z;
      const f32x2 mr01 = {bflo(u0), bflo(u1)}, rr01 = {bfhi(u0), bfhi(u1)}, mr23 = {bflo(u2), bflo(u3)}, rr23 = {bfhi(u2), bfhi(u3)};
      const f32x2 ylo = (a0.xy + a1.xy) + (a2.xy + a3.xy), yhi = (a0.zw + a1.zw) + (a2.zw + a3.zw);
      const float s0_ = al ? ylo.x : yhi.x, s1_ = al ? ylo.y : yhi.y;
      const float r0_ = asf(__builtin_amdgcn_update_dpp(0, asu(s0_), 0xB1, 0xf, 0xf, true));
      const float r1_ = asf(__builtin_amdgcn_update_dpp(0, asu(s1_), 0xB1, 0xf, 0xf, true));
      const f32x2 y01 = {al ? r0_ : ylo.x, al ? yhi.x : r0_}, y23 = {al ? r1_ : ylo.y, al ? yhi.y : r1_};
      const unsigned k1 = kvr.y, k0 = kvr.x;
      const f32x2 x01 = up2(xt.x), x23 = up2(xt.y);
      const f32x2 dd = {dch, dch};
      const f32x2 t01 = fma2(f32x2{bfhi(k1), bflo(k1)}, dd, y01), t23 = fma2(f32x2{bfhi(k0), bflo(k0)}, dd, y23);
      const f32x2 h01 = fma2(fma2(x01, rr01, mr01), t01, x01), h23 = fma2(fma2(x23, rr23, mr23), t23, x23);
      const int sl = (s & 63) >> 2;
      __builtin_amdgcn_raw_buffer_store_b64(v2u{pk(h01.x, h01.y), pk(h23.x, h23.y)}, rs,
                                            OFF_HT + ((s >> 6) * D + bch) * 128 + ((sl ^ (((bch >> 1) & 1) << 3)) << 3), 0, SC1);
    };
    {
      v4u af[8], bf[8], ag[8], bg[8];
      f32x4 acc[4];
#pragma unroll
      for (int i = 0; i < 4; ++i) acc[i] = f32x4{0.f, 0.f, 0.f, 0.f};
#pragma unroll
      for (int i = 0; i < 4; ++i) { bf[i] = *(LAS v4u*)(bb + 512 * (7 - i)); af[i] = *(LAS v4u*)(ab + 512 * i); }
#pragma unroll
      for (int i = 0; i < 4; ++i) { bg[i] = *(LAS v4u*)(bb - 64 + 512 * (7 - i)); ag[i] = *(LAS v4u*)(ab - 64 + 512 * i); }
      SCHED();
#pragma unroll
      for (int ip = 0; ip < 4; ++ip)
#pragma unroll
        for (int i = ip; i < 4; ++i) acc[i] = mfma16(af[i - ip], bf[ip], acc[i]);
#pragma unroll
      for (int i = 4; i < 8; ++i) { bf[i] = *(LAS v4u*)(bb + 512 * (7 - i)); af[i] = *(LAS v4u*)(ab + 512 * i); }
      SCHED();

#pragma unroll
      for (int i = 0; i < 4; ++i)
#pragma unroll
        for (int ip = 0; ip <= i; ++ip) acc[i] = mfma16(ag[i - ip], bg[ip], acc[i]);
      SCHED();
      asm volatile("s_waitcnt vmcnt(0)" : "+v"(xt0), "+v"(xt1) :: "memory");
      if (w >= 4 && bid < 224) {
#pragma unroll
        for (int cc = 0; cc < 2; ++cc) {
          const int v = w & 3;
          const int off = v * 2048 + (16 * cc + 8 * hh + tq) * 64 + 32 * blk + 16 * (tp & 1) + 8 * (tp >> 1);
          const v4u f1 = trfrag(L + A_W1 + off, 256);
          const v4u f2 = trfrag(L + A_W2 + off, 256);
          const int k16 = 2 * (bid / 14) + cc, nt = bid % 14;
          st16(f1, rs, OFF_W1 + (((nt * 8 + (k16 >> 4) * 4 + v) * 16) + (k16 & 15)) * 1024 + lane * 16);
          const int ka = 2 * (bid >> 2) + cc, dt = 2 * (bid & 3) + (v >> 1);
          st16(f2, rs, OFF_W2 + (((dt * 8 + ka / 14) * 28) + (ka % 14) * 2 + (v & 1)) * 1024 + lane * 16);
        }
      }
#pragma unroll
      for (int i = 0; i < 4; ++i) red[(rw * 8 + i) * 64 + lane] = acc[i];
      BARRIER();
      STAMPT(21);
      f32x4 ac2[4];
#pragma unroll
      for (int i = 0; i < 4; ++i) ac2[i] = f32x4{0.f, 0.f, 0.f, 0.f};
      bfinish(0, xt0);
      STAMPT(26);
      SCHED();
#pragma unroll
      for (int ip = 0; ip < 8; ++ip) {
#pragma unroll
        for (int i = (ip > 4 ? ip : 4); i < 8; ++i) ac2[i - 4] = mfma16(af[i - ip], bf[ip], ac2[i - 4]);
        SCHED();
        if (ip < 4) { ag[4 + ip] = *(LAS v4u*)(ab - 64 + 512 * (4 + ip)); bg[4 + ip] = *(LAS v4u*)(bb - 64 + 512 * (7 - 4 - ip)); }
        SCHED();
      }
      WAIT_V(0);
      lds_count_flag8(L, 4, lane, tag, rs, OFF_FB0 + bid * 16);
      asm volatile("global_load_dwordx4 %0, %1, %2" : "=v"(b1a) : "v"((unsigned)(128 * d_nt + 32 * nq + 8 * hh) * 2), "s"(p.b1) : "memory");
      asm volatile("global_load_dwordx4 %0, %1, %2 offset:32" : "=v"(b1b) : "v"((unsigned)(128 * d_nt + 32 * nq + 8 * hh) * 2), "s"(p.b1) : "memory");
      SCHED();
#pragma unroll
      for (int i = 4; i < 8; ++i) {
#pragma unroll
        for (int ip = 0; ip <= i; ++ip) {
          ac2[i - 4] = mfma16(ag[i - ip], bg[ip], ac2[i - 4]);
        }
        SCHED();
        if (i > 4) red[(rw * 8 + 4 + i - 5) * 64 + lane] = ac2[i - 5];
        SCHED();
      }
      red[(rw * 8 + 7) * 64 + lane] = ac2[3];
    }
    BARRIER();
    STAMPT(6);
    if (w == 0) LOOK_ISSUE(OFF_FB0 + xcd * 4096 + lane * 16);
    bfinish(1, xt1);
    STAMPT(7);
    if (w == 0) LOOK_CHECK(1, OFF_FB0 + xcd * 4096 + lane * 16);
    STAMPT(8);
  }

  {
    BARRIER();
    STAMPT(9);
    const GAS unsigned char* tsrc = (const GAS unsigned char*)p.ws + OFF_HT + (size_t)d_mt * 65536 + w * 8192 + lane * 16;
    const unsigned w2off = OFF_W2 + (e_dt * 8 + w) * 28 * 1024 + lane * 16;
    auto second_wait = [&]() {
      if (w == 0) { poll4(rs, OFF_FB1 + xcd * 4096 + lane * 16, tag); lds_set(L, 0); }
      lds_wait(L, 0);
    };
    auto second_res = [&]() {
      const int rr = 16 * (w & 3) + (lane >> 2);
      const int dch2 = 64 * e_dt + rr;
      dma1s((const GAS unsigned char*)p.ws + OFF_HT + ((16 * (w >> 2) + e_mt) * D + dch2) * 128 + (((unsigned)e_j ^ ((dch2 >> 1) & 1)) << 6) + 16 * (lane & 3),
            LB + E_RES + w * 1024);
    };
    if (d_act) { dma4s(tsrc, LB + D_TILE + w * 8192); dma4s(tsrc + 4096, LB + D_TILE + w * 8192 + 4096); }
    if (d_act) { WAIT_V(8); } else { WAIT_V(0); }
    lds_count_flag8(L, 5, lane, tag, rs, OFF_FB1 + bid * 16);
    if (d_act) {
      asm volatile("" : "+v"(b1a), "+v"(b1b) :: "memory");
#pragma unroll
      for (int i = 0; i < 8; ++i) w1load(i);
      SCHED();
      const unsigned basel = (unsigned)(D_TILE + (256 * kh + 8 * hh + tq) * 128 + 32 * blk + 8 * tp);
      const unsigned fsw = ((tq >> 1) & 1) << 6;
      const unsigned adr[2] = {basel + ((unsigned)(kh << 6) ^ fsw), basel + ((unsigned)((kh ^ 1) << 6) ^ fsw)};
      f32x16 acc[2][2];
      auto fraddr = [&](int c, int m) { return L + (adr[m & 1] + (unsigned)(c * 65536 + 8192 * ((((m >> 1) >> 2) + nq) & 3)) + 2048 * ((m >> 1) & 3)); };
      float xg[16];
      unsigned og[8];
      auto hstore = [&](int c) {
        const unsigned ho = OFF_HF + ((32 * c + 2 * d_mt + kh) * 112 + 8 * d_nt + 2 * nq) * 1024 + lane * 16;
        st16(v4u{og[0], og[1], og[2], og[3]}, rs, ho);
        st16(v4u{og[4], og[5], og[6], og[7]}, rs, ho + 1024);
      };
      float ge[16];
      auto gelu_1 = [&](int e) { const float x = xg[e]; ge[e] = __builtin_amdgcn_exp2f(x * __builtin_fmaf(x * x, -0.10294324f, -2.3022082f)); asm volatile("" : "+v"(ge[e])); };
      auto gelu_2 = [&](int e) { ge[e] = __builtin_amdgcn_rcpf(ge[e] + 1.0f); asm volatile("" : "+v"(ge[e])); };
      auto gelu_3 = [&](int e) {
        float y = xg[e] * ge[e];
        asm volatile("" : "+v"(y));
        xg[e] = y;
        if (e & 1) og[e >> 1] = pk(xg[e - 1], xg[e]);
      };
      auto stats = [&](int c) {
          float s1[2][2] = {{0.f, 0.f}, {0.f, 0.f}}, s2[2][2] = {{0.f, 0.f}, {0.f, 0.f}};
          v4u sf[8];
#pragma unroll
          for (int n = 0; n < 8; ++n) sf[n] = trfrag(fraddr(c, n), 512);
#pragma unroll
          for (int n = 0; n < 8; ++n) {
            const int r = n & 1;
            const v4u hf = sf[n];
            s1[r][0] = dot2(hf.x, ONES, s1[r][0]); s2[r][0] = dot2(hf.x, hf.x, s2[r][0]);
            s1[r][1] = dot2(hf.y, ONES, s1[r][1]); s2[r][1] = dot2(hf.y, hf.y, s2[r][1]);
            s1[r][0] = dot2(hf.z, ONES, s1[r][0]); s2[r][0] = dot2(hf.z, hf.z, s2[r][0]);
            s1[r][1] = dot2(hf.w, ONES, s1[r][1]); s2[r][1] = dot2(hf.w, hf.w, s2[r][1]);
          }
#pragma unroll
          for (int r = 0; r < 2; ++r) {
            const float a = halfsum(s1[r][0] + s1[r][1]), b = halfsum(s2[r][0] + s2[r][1]);
            if (lane < 32) *(LAS f32x2*)(L + D_STAT + (((c * 8 + w) * 2 + ((r + kh) & 1)) * 32 + lane) * 8) = f32x2{a, b};
          }
        };
      auto mstep = [&](int c) {
#pragma unroll
        for (int r = 0; r < 2; ++r)
#pragma unroll
          for (int e = 0; e < 16; ++e) acc[c][r][e] = 0.f;
        v4u fr[4];
#pragma unroll
        for (int n = 0; n < 3; ++n) fr[n] = trfrag(fraddr(c, n), 512);
        SCHED();
#pragma unroll
        for (int n = 0; n < 32; ++n) {
          const int i = n >> 1, r = n & 1;
          if (c == 0 && r == 0) {
            if (n < 16) { w1load(8 + i); WAITF(8, wf1[i]); }
            if (n == 16) {
              WAITF(7, wf1[8]);
              if (w == 0) { LOOK_CHECK(4, OFF_FB1 + xcd * 4096 + lane * 16); lds_set(L, 0); }
              lds_wait(L, 0);
            }
            if (n == 18) WAITF(7, wf1[9]);
            if (n == 20) WAITF(7, wf1[10]);
            if (n == 22) WAITF(7, wf1[11]);
            if (n == 24) WAITF(7, wf1[12]);
            if (n == 26) WAITF(7, wf1[13]);
            if (n == 28) WAITF(7, wf1[14]);
            if (n == 30) WAITF(7, wf1[15]);
          }
          if (c == 0 && n == 7 && w == 0) LOOK_ISSUE(OFF_FB1 + xcd * 4096 + lane * 16);
          if (c == 0 && n >= 16 && (n & 1) == 0) dma1s(tsrc + 16 * 65536 + ((n - 16) >> 1) * 1024, LB + D_TILE + 65536 + w * 8192 + ((n - 16) >> 1) * 1024);
          if (c == 1 && n == 1) second_res();
          if (n + 3 < 32) fr[(n + 3) & 3] = trfrag(fraddr(c, n + 3), 512);
          acc[c][r] = mfma32(wf1[i], fr[n & 3], acc[c][r]);
          if (SEQ == 0 && c == 1) {
            if (n >= 2 && n < 18) gelu_1(n - 2);
            if (n >= 4 && n < 20) gelu_2(n - 4);
            if (n >= 6 && n < 22) gelu_3(n - 6);
            if (n == 23) hstore(0);
          }
          if (c == 1 && (n & 1) == 0) wf2[n >> 1] = ld16(rs, w2off + (n >> 1) * 1024);
          SCHED();
        }
        if (c == 0) {
          float cs0 = 0.f, cs1 = 0.f;
#pragma unroll
          for (int i = 0; i < 16; ++i) {
            const v4u wv = wf1[i];
            cs0 = dot2(wv.x, ONES, cs0); cs1 = dot2(wv.y, ONES, cs1); cs0 = dot2(wv.z, ONES, cs0); cs1 = dot2(wv.w, ONES, cs1);
          }
          const float t = halfsum(cs0 + cs1);
          if (lane < 32) *(LAS float*)(L + D_CS + (w * 32 + lane) * 4) = t;
        }
      };
      auto xwrite = [&](int c) {
        LAS f32x4* ex = (LAS f32x4*)(L + D_TILE + c * 32768) + w * 256 + lane;
#pragma unroll
        for (int qd = 0; qd < 4; ++qd) ex[qd * 64] = f32x4{acc[c][1][4 * qd], acc[c][1][4 * qd + 1], acc[c][1][4 * qd + 2], acc[c][1][4 * qd + 3]};
      };
      auto xpre = [&](int c) {
        LAS f32x4* ex = (LAS f32x4*)(L + D_TILE + c * 32768) + (w ^ 4) * 256 + lane;
        f32x2 sa = {0.f, 0.f};
#pragma unroll
        for (int ww = 0; ww < 8; ++ww) sa += *(LAS f32x2*)(L + D_STAT + (((c * 8 + ww) * 2 + kh) * 32 + (lane & 31)) * 8);
        const float mean = sa.x * (1.0f / D);
        float var = __builtin_fmaf(-mean, mean, sa.y * (1.0f / D));
        var = var > 0.f ? var : 0.f;
        const float r1 = __builtin_amdgcn_rsqf(var + 1e-5f);
        const float rstd = r1 * __builtin_amdgcn_rsqf(__builtin_fmaf(var * r1, r1, 1e-5f));
        const float nm = -rstd * mean;
        const f32x2 rs2 = {rstd, rstd}, nm2 = {nm, nm};
        LAS f32x4* c4 = (LAS f32x4*)(L + D_CS) + nq * 8 + hh;
        const unsigned bb[8] = {b1a.x, b1a.y, b1a.z, b1a.w, b1b.x, b1b.y, b1b.z, b1b.w};
#pragma unroll
        for (int qd = 0; qd < 4; ++qd) {
          const f32x4 pp = ex[qd * 64];
          const f32x4 cs = c4[2 * qd] + c4[32 + 2 * qd];
#pragma unroll
          for (int hf = 0; hf < 2; ++hf) {
            const int e = 2 * qd + hf;
            const f32x2 a = f32x2{acc[c][0][2 * e], acc[c][0][2 * e + 1]} + (hf ? pp.zw : pp.xy);
            const f32x2 x = fma2(rs2, a, fma2(nm2, hf ? cs.zw : cs.xy, up2(bb[e])));
            xg[2 * e] = x.x; xg[2 * e + 1] = x.y;
            if (SEQ == 0 && c == 0) { asm("" : "+v"(xg[2 * e])); asm("" : "+v"(xg[2 * e + 1])); }
          }
        }
      };
      auto gelu_all = [&]() {
#pragma unroll
        for (int e = 0; e < 8; ++e) {
          const f32x2 x = {xg[2 * e], xg[2 * e + 1]};
          const f32x2 pz = fma2(x * x, f32x2{-0.10294324f, -0.10294324f}, f32x2{-2.3022082f, -2.3022082f});
          const f32x2 z = x * pz;
          const f32x2 den = f32x2{__builtin_amdgcn_exp2f(z.x), __builtin_amdgcn_exp2f(z.y)} + f32x2{1.f, 1.f};
          const f32x2 y = x * f32x2{__builtin_amdgcn_rcpf(den.x), __builtin_amdgcn_rcpf(den.y)};
          og[e] = pk(y.x, y.y);
        }
      };
      WAIT_V(8);
      BARRIER();
      STAMPT(10);
      stats(0);
      BARRIER();
      SCHED();
      mstep(0);
      STAMPT(22);
      BARRIER();
      STAMPT(11);
      xwrite(0);
      WAIT_V(0);
      BARRIER();
      STAMPT(23);
      xpre(0);
      gelu_all();
      hstore(0);
      stats(1);
      BARRIER();
      STAMPT(24);
      mstep(1);
      STAMPT(25);
      xwrite(1);
      WAIT_V(17);
      lds_count_flag(L, 2, 8, tid, d_act, tag, rs, OFF_FD + d_mt * 256 + d_nt * 16);
      BARRIER();
      STAMPT(12);
      xpre(1);
      gelu_all();
      hstore(1);
      STAMPT(13);
    }
    asm volatile("" ::: "memory");
    if (!d_act) {
      second_wait();
      second_res();
#pragma unroll
      for (int i = 0; i < 16; ++i) wf2[i] = ld16(rs, w2off + i * 1024);
    }
  }

  {
    const int sbo = w >> 2, dbo = (w >> 1) & 1, hf = w & 1;
    if (w == 0) poll1(rs, OFF_FD + e_mt * 256 + (lane < 14 ? lane : 0) * 16, tag);
    const unsigned w2off = OFF_W2 + (e_dt * 8 + w) * 28 * 1024 + lane * 16;
#pragma unroll
    for (int i = 16; i < 28; ++i) wf2[i] = ld16(rs, w2off + i * 1024);
    const v4u b2v = *(const GAS v4u*)((const GAS char*)p.b2 + (unsigned)(64 * e_dt + 32 * dbo + 16 * hf + 8 * hh) * 2);
    SCHED();
    WAIT_V(13);
    BARRIER();
    if (d_act && tid == 0) st4(tag, rs, OFF_FD + 4096 + d_mt * 256 + d_nt * 16);
    STAMPT(14);
    const unsigned hoff0 = OFF_HF + ((2 * e_mt + e_j) * 112 + 14 * w) * 1024 + lane * 16, hoff1 = hoff0 + 32 * 112 * 1024;
    if (w == 0) { poll1(rs, OFF_FD + 4096 + e_mt * 256 + (lane < 14 ? lane : 0) * 16, tag); lds_set(L, 1); }
    v4u hf0[14], hf1[14];
#pragma unroll
    for (int k = 0; k < 14; ++k) hf0[k] = ld16(rs, hoff0 + k * 1024);
    SCHED();
    lds_wait(L, 1);
    STAMPT(15);
    LAS f32x4* red = (LAS f32x4*)(L + E_RED + w * 16384) + lane;
    {
      f32x16 acc[2];
#pragma unroll
      for (int e = 0; e < 16; ++e) { acc[0][e] = 0.f; acc[1][e] = 0.f; }
#pragma unroll
      for (int k = 0; k < 14; ++k) {
        acc[0] = mfma32(wf2[2 * k], hf0[k], acc[0]); acc[1] = mfma32(wf2[2 * k + 1], hf0[k], acc[1]);
        SCHED();
        hf1[k] = ld16(rs, hoff1 + k * 1024);
        SCHED();
      }
#pragma unroll
      for (int d = 0; d < 2; ++d)
#pragma unroll
        for (int qd = 0; qd < 4; ++qd) red[(d * 4 + qd) * 64] = f32x4{acc[d][4 * qd], acc[d][4 * qd + 1], acc[d][4 * qd + 2], acc[d][4 * qd + 3]};
    }
    STAMPT(16);
    {
      f32x16 acc[2];
#pragma unroll
      for (int e = 0; e < 16; ++e) { acc[0][e] = 0.f; acc[1][e] = 0.f; }
#pragma unroll
      for (int k = 0; k < 14; ++k) { acc[0] = mfma32(wf2[2 * k], hf1[k], acc[0]); acc[1] = mfma32(wf2[2 * k + 1], hf1[k], acc[1]); }
#pragma unroll
      for (int d = 0; d < 2; ++d)
#pragma unroll
        for (int qd = 0; qd < 4; ++qd) red[512 + (d * 4 + qd) * 64] = f32x4{acc[d][4 * qd], acc[d][4 * qd + 1], acc[d][4 * qd + 2], acc[d][4 * qd + 3]};
    }
    BARRIER();
    STAMPT(17);
    f32x2 o[4] = {{0.f, 0.f}, {0.f, 0.f}, {0.f, 0.f}, {0.f, 0.f}};
#pragma unroll
    for (int ww = 0; ww < 8; ++ww) {
      LAS f32x4* rp = (LAS f32x4*)(L + E_RED + ww * 16384) + sbo * 512 + (dbo * 4 + 2 * hf) * 64 + lane;
      const f32x4 a = rp[0], b = rp[64];
      o[0] += a.xy; o[1] += a.zw; o[2] += b.xy; o[3] += b.zw;
    }
    const v4u hr = trfrag(L + E_RES + sbo * 4096 + (32 * dbo + 16 * hf + 8 * hh + tq) * 64 + 32 * blk + 8 * tp, 256);
    o[0] += up2(b2v.x) + up2(hr.x); o[1] += up2(b2v.y) + up2(hr.y);
    o[2] += up2(b2v.z) + up2(hr.z); o[3] += up2(b2v.w) + up2(hr.w);
    const v4u ov = {pk(o[0].x, o[0].y), pk(o[1].x, o[1].y), pk(o[2].x, o[2].y), pk(o[3].x, o[3].y)};
    {
      const rsrc_t ro = __builtin_amdgcn_make_buffer_rsrc((void*)p.out, 0, S * D * 2, 0x00020000);
      st16(ov, ro, (unsigned)((1024 * sbo + 64 * e_mt + 32 * e_j + (lane & 31)) * D + 64 * e_dt + 32 * dbo + 16 * hf + 8 * hh) * 2);
    }
    if (bid == 0 && tid == 0) st4(tag, rs, OFF_EP);
    STAMPT(18);
  }
}

extern "C" void kernel_launch(void* const* d_in, const int* in_sizes, int n_in, void* d_out, int out_size, void* d_ws,
                              size_t ws_size, hipStream_t stream) {
  Params p;
  p.q = (const u16*)d_in[0]; p.k = (const u16*)d_in[1]; p.v = (const u16*)d_in[2];
  p.shk = (const u16*)d_in[3]; p.ssk = (const u16*)d_in[4]; p.ssd = (const u16*)d_in[5];
  p.w1 = (const u16*)d_in[7]; p.b1 = (const u16*)d_in[8]; p.w2 = (const u16*)d_in[9]; p.b2 = (const u16*)d_in[10];
  p.out = (u16*)d_out; p.ws = (unsigned char*)d_ws;
  fused<<<256, 512, 0, stream>>>(p);
}
```

```cpp
#include <hip/hip_runtime.h>
#include <hip/hip_bf16.h>

#define GAS __attribute__((address_space(1)))
#define LAS __attribute__((address_space(3)))
typedef unsigned short u16;
typedef unsigned v4u __attribute__((ext_vector_type(4)));
typedef unsigned v2u __attribute__((ext_vector_type(2)));
typedef float f32x16 __attribute__((ext_vector_type(16)));
typedef float f32x4 __attribute__((ext_vector_type(4)));
typedef float f32x2 __attribute__((ext_vector_type(2)));
typedef short s16x4 __attribute__((ext_vector_type(4)));
typedef short bf16x8 __attribute__((ext_vector_type(8)));
typedef __bf16 bf2 __attribute__((ext_vector_type(2)));
typedef __amdgpu_buffer_rsrc_t rsrc_t;

constexpr int S = 2048, D = 512, F = 1792;
constexpr unsigned OFF_EP = 0;
constexpr unsigned OFF_FA1 = 4096;
constexpr unsigned OFF_FD = 8192;
constexpr unsigned OFF_FB0 = 16384;
constexpr unsigned OFF_FB1 = OFF_FB0 + 32768;
constexpr unsigned OFF_XR = 262144;
constexpr unsigned OFF_XT = OFF_XR + D * S * 2;
constexpr unsigned OFF_HT = OFF_XT + D * S * 2;
constexpr unsigned OFF_ST = OFF_HT + D * S * 2;
constexpr unsigned OFF_HS = OFF_ST + 16384;
constexpr unsigned OFF_CS = OFF_HS + 32768;
constexpr unsigned OFF_W1 = OFF_CS + 16384;
constexpr unsigned OFF_W2 = OFF_W1 + D * F * 2;
constexpr unsigned OFF_HF = OFF_W2 + D * F * 2;
constexpr unsigned WS_END = OFF_HF + S * F * 2;
constexpr int SPIN_CAP = 1 << 16;

struct Params {
  const u16 *q, *k, *v, *shk, *ssk, *ssd, *w1, *b1, *w2, *b2;
  u16* out;
  unsigned char* ws;
};

__device__ __forceinline__ float asf(unsigned u) { return __builtin_bit_cast(float, u); }
__device__ __forceinline__ unsigned asu(float f) { return __builtin_bit_cast(unsigned, f); }
__device__ __forceinline__ float bflo(unsigned u) { return asf(u << 16); }
__device__ __forceinline__ float bfhi(unsigned u) { return asf(u & 0xffff0000u); }
__device__ __forceinline__ unsigned pk(float lo, float hi) {
  f32x2 f = {lo, hi};
  return __builtin_bit_cast(unsigned, __builtin_convertvector(f, bf2));
}
__device__ __forceinline__ f32x2 up2(unsigned u) { return f32x2{bflo(u), bfhi(u)}; }
__device__ __forceinline__ f32x2 fma2(f32x2 a, f32x2 b, f32x2 c) { return __builtin_elementwise_fma(a, b, c); }
__device__ __forceinline__ f32x4 mfma16(v4u a, v4u b, f32x4 c) {
  return __builtin_amdgcn_mfma_f32_16x16x32_bf16(__builtin_bit_cast(bf16x8, a), __builtin_bit_cast(bf16x8, b), c, 0, 0, 0);
}
__device__ __forceinline__ f32x16 mfma32(v4u a, v4u b, f32x16 c) {
  return __builtin_amdgcn_mfma_f32_32x32x16_bf16(__builtin_bit_cast(bf16x8, a), __builtin_bit_cast(bf16x8, b), c, 0, 0, 0);
}
__device__ __forceinline__ v2u trread(LAS unsigned char* p) {
  return __builtin_bit_cast(v2u, __builtin_amdgcn_ds_read_tr16_b64_v4i16((LAS s16x4*)p));
}
__device__ __forceinline__ v4u trfrag(LAS unsigned char* p, int second) {
  const v2u a = trread(p), b = trread(p + second);
  return v4u{a.x, a.y, b.x, b.y};
}
__device__ __forceinline__ float dot2(unsigned a, unsigned b, float c) {
  return __builtin_amdgcn_fdot2_f32_bf16(__builtin_bit_cast(bf2, a), __builtin_bit_cast(bf2, b), c, false);
}
constexpr int SC1 = 16;
#define DMA_HEAD "s_mov_b32 m0, %1\n\ts_nop 0\n\t"
#define DMA_P(o, f) "global_load_lds_dwordx4 %0, off offset:" #o f "\n\t"
__device__ __forceinline__ void dma4(const GAS void* g, unsigned lds) {
  asm volatile(DMA_HEAD DMA_P(0, "") DMA_P(1024, "") DMA_P(2048, "") DMA_P(3072, "") :: "v"(g), "s"(lds) : "memory");
}
__device__ __forceinline__ void dma3(const GAS void* g, unsigned lds) {
  asm volatile(DMA_HEAD DMA_P(0, "") DMA_P(1024, "") DMA_P(2048, "") :: "v"(g), "s"(lds) : "memory");
}
__device__ __forceinline__ void dma1(const GAS void* g, unsigned lds) {
  asm volatile(DMA_HEAD DMA_P(0, "") :: "v"(g), "s"(lds) : "memory");
}
__device__ __forceinline__ void dma4s(const GAS void* g, unsigned lds) {
  asm volatile(DMA_HEAD DMA_P(0, " sc1") DMA_P(1024, " sc1") DMA_P(2048, " sc1") DMA_P(3072, " sc1") :: "v"(g), "s"(lds) : "memory");
}
__device__ __forceinline__ void dma3s(const GAS void* g, unsigned lds) {
  asm volatile(DMA_HEAD DMA_P(0, " sc1") DMA_P(1024, " sc1") DMA_P(2048, " sc1") :: "v"(g), "s"(lds) : "memory");
}
__device__ __forceinline__ void dma2s(const GAS void* g, unsigned lds) {
  asm volatile(DMA_HEAD DMA_P(0, " sc1") DMA_P(1024, " sc1") :: "v"(g), "s"(lds) : "memory");
}
__device__ __forceinline__ void dma1s(const GAS void* g, unsigned lds) {
  asm volatile(DMA_HEAD DMA_P(0, " sc1") :: "v"(g), "s"(lds) : "memory");
}
__device__ __forceinline__ v4u ld16(rsrc_t rs, unsigned off) { return __builtin_amdgcn_raw_buffer_load_b128(rs, off, 0, SC1); }
__device__ __forceinline__ void st16(v4u v, rsrc_t rs, unsigned off) { __builtin_amdgcn_raw_buffer_store_b128(v, rs, off, 0, SC1); }
__device__ __forceinline__ void st4(unsigned v, rsrc_t rs, unsigned off) { __builtin_amdgcn_raw_buffer_store_b32(v, rs, off, 0, SC1); }
template <int CTRL, int RM>
__device__ __forceinline__ float dppadd(float x) {
  return x + asf(__builtin_amdgcn_update_dpp(0, asu(x), CTRL, RM, 0xf, true));
}
__device__ __forceinline__ float wavesum(float x) {
  x = dppadd<0x111, 0xf>(x); x = dppadd<0x112, 0xf>(x); x = dppadd<0x114, 0xf>(x); x = dppadd<0x118, 0xf>(x);
  x = dppadd<0x142, 0xa>(x); x = dppadd<0x143, 0xc>(x);
  return x;
}
__device__ __forceinline__ float halfsum(float x) {
  const unsigned u = asu(x);
  auto r = __builtin_amdgcn_permlane32_swap(u, u, false, false);
  const unsigned r0 = r[0], r1 = r[1];
  return asf(r0) + asf(r1);
}
#define WAIT_V(n) asm volatile("s_waitcnt vmcnt(" #n ")" ::: "memory")
#define BARRIER() do { asm volatile("s_waitcnt lgkmcnt(0)" ::: "memory"); __builtin_amdgcn_s_barrier(); asm volatile("" ::: "memory"); } while (0)
#define SCHED() __builtin_amdgcn_sched_barrier(0)
constexpr unsigned ONES = 0x3f803f80u;

#define STAMPT(idx)

#define CODEPF 0
constexpr unsigned CODE_BYTES = 22016;
constexpr int LDS_BYTES = 163840;
constexpr int A_K = 0, A_V = A_K + 68 * 128, A_Q = A_V + 8192, A_END = A_Q + 8192;
constexpr int B_HCS = 4160, B_XCS = 4640;
constexpr int L_FLAG = LDS_BYTES - 64;
constexpr int B_XC = L_FLAG - 4 * B_XCS, B_HC = B_XC - 8 * B_HCS;
constexpr int B_RED = 0, B_ST = 65536, A_W1 = B_ST + 16384, A_W2 = A_W1 + 8192;
constexpr int D_TILE = 0, D_CSG = 131072, D_CB = D_CSG + 1024, D_END = D_CB + 1024;
constexpr int E_RED = 0, E_RES = 143360;
static_assert(A_W2 + 8192 <= B_HC && A_END <= 65536 && B_HC % 16 == 0 && D_END <= E_RES && E_RES + 8192 <= L_FLAG, "");

__device__ __forceinline__ void poll4(rsrc_t rs, unsigned off, unsigned tag) {
  for (int spins = 0; spins < SPIN_CAP; ++spins) {
    const unsigned f0 = __builtin_amdgcn_raw_buffer_load_b32(rs, off, 0, SC1), f1 = __builtin_amdgcn_raw_buffer_load_b32(rs, off + 1024, 0, SC1);
    const unsigned f2 = __builtin_amdgcn_raw_buffer_load_b32(rs, off + 2048, 0, SC1), f3 = __builtin_amdgcn_raw_buffer_load_b32(rs, off + 3072, 0, SC1);
    if (__builtin_amdgcn_ballot_w64(((f0 ^ tag) | (f1 ^ tag) | (f2 ^ tag) | (f3 ^ tag)) != 0u) == 0ull) break;
    __builtin_amdgcn_s_sleep(1);
    asm volatile("" ::: "memory");
  }
}
__device__ __forceinline__ void poll1(rsrc_t rs, unsigned off, unsigned tag) {
  for (int spins = 0; spins < SPIN_CAP; ++spins) {
    const unsigned f = __builtin_amdgcn_raw_buffer_load_b32(rs, off, 0, SC1);
    if (__builtin_amdgcn_ballot_w64(f == tag) == ~0ull) break;
    __builtin_amdgcn_s_sleep(1);
    asm volatile("" ::: "memory");
  }
}
__device__ __forceinline__ void lds_set(LAS unsigned char* L, int idx) {
  asm volatile("" ::: "memory");
  *(volatile LAS unsigned*)(L + L_FLAG + 4 * idx) = 1u;
}
__device__ __forceinline__ void lds_wait(LAS unsigned char* L, int idx) {
  for (int spins = 0; spins < (SPIN_CAP << 4); ++spins) {
    if (__builtin_amdgcn_readfirstlane(*(volatile LAS unsigned*)(L + L_FLAG + 4 * idx)) != 0) break;
    __builtin_amdgcn_s_sleep(1);
  }
  asm volatile("" ::: "memory");
}
#define GLD(dst, voff, base, imm) asm volatile("global_load_dwordx4 %0, %1, %2 offset:" #imm " sc1" : "=v"(dst) : "v"(voff), "s"(base) : "memory")
__device__ __forceinline__ void lds_count_flag(LAS unsigned char* L, int idx, unsigned n, int tid, bool act, unsigned tag, rsrc_t rs, unsigned off) {
  if ((tid & 63) == 0) {
    const unsigned old = __hip_atomic_fetch_add((LAS unsigned*)(L + L_FLAG + 4 * idx), 1u, __ATOMIC_RELAXED, __HIP_MEMORY_SCOPE_WORKGROUP);
    if (old == n - 1 && act) st4(tag, rs, off);
  }
}
__device__ __forceinline__ bool lds_count_flag8(LAS unsigned char* L, int idx, int lane, unsigned tag, rsrc_t rs, unsigned off) {
  unsigned old = 0;
  if (lane == 0) old = __hip_atomic_fetch_add((LAS unsigned*)(L + L_FLAG + 4 * idx), 1u, __ATOMIC_RELAXED, __HIP_MEMORY_SCOPE_WORKGROUP);
  const bool last = __builtin_amdgcn_readfirstlane(old) == 7;
  if (last && lane < 8) st4(tag, rs, off + lane * 4096);
  return last;
}
#define LOOK_ISSUE(off) do { const GAS char* fp_ = (const GAS char*)p.ws + (off); \
    asm volatile("global_load_dword %0, %1, off sc1" : "=v"(pf0) : "v"(fp_) : "memory"); \
    asm volatile("global_load_dword %0, %1, off offset:1024 sc1" : "=v"(pf1) : "v"(fp_) : "memory"); \
    asm volatile("global_load_dword %0, %1, off offset:2048 sc1" : "=v"(pf2) : "v"(fp_) : "memory"); \
    asm volatile("global_load_dword %0, %1, off offset:3072 sc1" : "=v"(pf3) : "v"(fp_) : "memory"); } while (0)
#define LOOK_CHECK(n, off) do { asm volatile("s_waitcnt vmcnt(" #n ")" : "+v"(pf0), "+v"(pf1), "+v"(pf2), "+v"(pf3) :: "memory"); \
    if (__builtin_amdgcn_ballot_w64(pf0 == tag && pf1 == tag && pf2 == tag && pf3 == tag) != ~0ull) poll4(rs, (off), tag); } while (0)
#define WAITF(n, x) asm volatile("s_waitcnt vmcnt(" #n ")" : "+v"(x) :: "memory")
#define ALOAD(op, dst, ptr) asm volatile(op " %0, %1, off" : "=v"(dst) : "v"(ptr) : "memory")

__global__ void __launch_bounds__(512, 2) fused(Params p) {
  __shared__ __attribute__((aligned(16))) unsigned char smem[LDS_BYTES];
  LAS unsigned char* L = (LAS unsigned char*)smem;
  const unsigned LB = (unsigned)(size_t)L;
  const int tid = threadIdx.x, lane = tid & 63, w = __builtin_amdgcn_readfirstlane(tid >> 6);
  const int bid = blockIdx.x, xcd = bid & 7, bix = bid >> 3;
  const rsrc_t rs = __builtin_amdgcn_make_buffer_rsrc((void*)p.ws, 0, (int)WS_END, 0x00020000);
  const int g = lane >> 4, i16 = lane & 15, tq = i16 >> 2, tp = i16 & 3;
  const int hh = lane >> 5, blk = g & 1;
  STAMPT(0);

  const int cj = xcd * 32 + bix;
  const bool d_act = bix < 28;
  const int d_mt = 4 * (xcd >> 1) + (d_act ? bix / 7 : 0), d_nt = 7 * (xcd & 1) + bix % 7;
  const int e_mt = 2 * xcd + (bix >> 4), e_j = (bix >> 3) & 1, e_dt = bix & 7;
  const int chl = w >> 2, q4 = w & 3, bch = 2 * cj + chl;

  v4u wf1[16];
  unsigned tag;
  unsigned hval, dv;
  {
    const int b = xcd, a = bix;
    const int ch = 64 * b + 16 * (w & 3) + i16, seg = 4 * (w >> 2) + g;
    const int srow = 64 * a + 8 * b + w;
    v2u shw; v4u qs;
    {
      const int rl = lane >> 3, sl = lane & 7;
      const int rhok = 4 + 8 * w + rl, rhov = 8 * w + rl;
      const unsigned fk = ((rhok >> 1) & 1) | (((rhok >> 3) & 1) << 1), fv = ((rhov >> 1) & 1) | (((rhov >> 3) & 1) << 1);
      const unsigned rowoff = ((64 * a + 8 * w + rl) * D + 64 * b) * 2 + 16 * (sl & 1);
      const unsigned uk = rowoff + 32 * ((sl >> 1) ^ fk), uv = rowoff + 32 * ((sl >> 1) ^ fv);
      if (w == 0 && a > 0 && lane < 32)
        dma1((const GAS char*)p.k + (((64 * a - 4 + rl) * D + 64 * b) * 2 + 16 * (sl & 1) + 32 * ((sl >> 1) ^ ((rl >> 1) & 1))), LB + A_K);
      dma1((const GAS char*)p.k + uk, LB + A_K + 512 + w * 1024);
      dma1((const GAS char*)p.v + uv, LB + A_V + w * 1024);
      ALOAD("global_load_dwordx2", shw, (const GAS char*)p.shk + (unsigned)ch * 8);
      dma1((const GAS char*)p.q + uv, LB + A_Q + w * 1024);
      asm volatile("global_load_dword %0, %1, off sc1" : "=v"(tag) : "v"((const GAS char*)p.ws + OFF_EP) : "memory");
      ALOAD("global_load_dwordx4", qs, (const GAS char*)p.q + (unsigned)(srow * D + 8 * lane) * 2);
      {
        const int hidx = (lane & 31) + 2 * q4 - 30;
        ALOAD("global_load_ushort", hval, (const GAS u16*)(p.ssk + (size_t)bch * S + (hidx < 0 ? 0 : hidx)));
        ALOAD("global_load_ushort", dv, (const GAS u16*)(p.ssd + bch));
      }
    }
    if (a == 0 && tid < 32) *(LAS v4u*)(L + A_K + 16 * tid) = v4u{0, 0, 0, 0};
    if (tid < 16) *(LAS unsigned*)(L + L_FLAG + 4 * tid) = 0u;
    if (tid < 128) *(LAS v4u*)(L + B_XC + (tid >> 5) * B_XCS + 4096 + 16 * (tid & 31)) = v4u{0, 0, 0, 0};
    asm volatile("s_waitcnt vmcnt(4)" : "+v"(shw) :: "memory");
    BARRIER();
    STAMPT(1);
    const int s0 = 64 * a + 8 * seg;
    const int r0 = 8 * seg + tq;
    const unsigned fr0 = (r0 >> 1) & 1, sg = seg & 1;
    const unsigned ua = ((w & 3) ^ (fr0 | (sg << 1))) * 32 + 8 * tp, ub = ((w & 3) ^ (fr0 | ((sg ^ 1) << 1))) * 32 + 8 * tp;
    {
      LAS unsigned char* kb = L + A_K + r0 * 128;
      const v2u k0 = trread(kb + ua), k1 = trread(kb + 512 + ua), k2 = trread(kb + 1024 + ub);
      LAS unsigned char* vb = L + A_V + r0 * 128 + ua;
      const v2u v0 = trread(vb), v1 = trread(vb + 512);
      LAS unsigned char* qb = L + A_Q + r0 * 128 + ua;
      const v2u q0 = trread(qb), q1 = trread(qb + 512);
      const unsigned E[6] = {k0.x, k0.y, k1.x, k1.y, k2.x, k2.y};
      unsigned O[5];
#pragma unroll
      for (int i = 0; i < 5; ++i) O[i] = __builtin_amdgcn_alignbit(E[i + 1], E[i], 16);
      const unsigned shx = shw.x, shy = shw.y;
      const unsigned w32 = __builtin_amdgcn_alignbit(shy, shy, 16);
      const unsigned w10 = __builtin_amdgcn_alignbit(shx, shx, 16);
      float ks[8];
#pragma unroll
      for (int j = 0; j < 8; ++j) {
        const unsigned pa = (j & 1) ? E[(j + 1) >> 1] : O[j >> 1];
        const unsigned pb = (j & 1) ? E[(j + 3) >> 1] : O[(j + 2) >> 1];
        ks[j] = dot2(pb, w10, dot2(pa, w32, 0.f));
      }
      const f32x2 a0 = f32x2{ks[0], ks[1]} * up2(v0.x), a1 = f32x2{ks[2], ks[3]} * up2(v0.y);
      const f32x2 a2 = f32x2{ks[4], ks[5]} * up2(v1.x), a3 = f32x2{ks[6], ks[7]} * up2(v1.y);
      const v4u xr = {pk(a3.y, a3.x), pk(a2.y, a2.x), pk(a1.y, a1.x), pk(a0.y, a0.x)};
      st16(xr, rs, OFF_XR + (ch * S + (S - 8 - s0)) * 2);
      st16(v4u{q0.x, q0.y, q1.x, q1.y}, rs, OFF_XT + (ch * S + s0) * 2);
    }
    asm volatile("s_waitcnt vmcnt(2)" : "+v"(qs), "+v"(tag), "+v"(hval), "+v"(dv) :: "memory");
    tag += 1u;
    {
      float s1 = dot2(qs.x, ONES, dot2(qs.y, ONES, 0.f)) + dot2(qs.z, ONES, dot2(qs.w, ONES, 0.f));
      float s2 = dot2(qs.x, qs.x, dot2(qs.y, qs.y, 0.f)) + dot2(qs.z, qs.z, dot2(qs.w, qs.w, 0.f));
      s1 = wavesum(s1); s2 = wavesum(s2);
      const float mean = s1 * (1.0f / D);
      float var = __builtin_fmaf(-mean, mean, s2 * (1.0f / D));
      var = var > 0.f ? var : 0.f;
      const float rstd = __builtin_amdgcn_rsqf(var + 1e-5f);
      if (lane == 63) __builtin_amdgcn_raw_buffer_store_b64(v2u{pk(-mean * rstd, rstd), tag}, rs, OFF_ST + 8 * srow, 0, SC1);
    }
    STAMPT(2);
    WAIT_V(1);
    BARRIER();
    if (tid == 0) st4(tag, rs, OFF_FA1 + (b * 32 + a) * 16);
    STAMPT(3);
    if (w >= 4) {
      const int w4 = w - 4, wb = bid < 224 ? bid : 0;
#pragma unroll
      for (int cc = 0; cc < 2; ++cc) {
        const int cw = 2 * w4 + cc;
        const u16* hrow = p.ssk + (size_t)(2 * cj + (cw >> 2)) * S;
        const int idx = 8 * (4 + lane) + 2 * (cw & 3) - 30;
        const unsigned lb = LB + B_HC + cw * B_HCS + 64;
        dma3((const GAS void*)(hrow + idx), lb);
        int idx2 = idx + 3 * 512;
        idx2 = idx2 > S - 8 ? S - 8 : idx2;
        dma1((const GAS void*)(hrow + idx2), lb + 3072);
      }
#pragma unroll
      for (int cc = 0; cc < 2; ++cc) {
        const int wr = 16 * cc + (lane >> 2);
        dma1((const GAS char*)p.w1 + (unsigned)((32 * (wb / 14) + wr) * F + 128 * (wb % 14) + 32 * w4 + 8 * (lane & 3)) * 2, LB + A_W1 + w4 * 2048 + cc * 1024);
        dma1((const GAS char*)p.w2 + (unsigned)((32 * (wb >> 2) + wr) * D + 128 * (wb & 3) + 32 * w4 + 8 * (lane & 3)) * 2, LB + A_W2 + w4 * 2048 + cc * 1024);
      }
    }
    if (w == 0) poll1(rs, OFF_FA1 + (xcd * 32 + (lane & 31)) * 16, tag);
    STAMPT(27);
    BARRIER();
    STAMPT(4);
  }

  v4u wf2[28];
  unsigned b1n;
  unsigned pf0 = 0, pf1 = 0, pf2 = 0, pf3 = 0;
  const int kh = w >> 2, nq = w & 3;
  const unsigned w1vo = OFF_W1 + (d_nt * 8 + w) * 16 * 1024 + lane * 16;
  auto w1load = [&](int i) {
    const unsigned vo = w1vo + (((i >> 2) + nq) & 3) * 4096;
    if ((i & 3) == 0) GLD(wf1[i], vo, p.ws, 0);
    if ((i & 3) == 1) GLD(wf1[i], vo, p.ws, 1024);
    if ((i & 3) == 2) GLD(wf1[i], vo, p.ws, 2048);
    if ((i & 3) == 3) GLD(wf1[i], vo, p.ws, 3072);
  };
  {
    const int c = i16, al = c & 1;
    const int sa = 256 * q4 + 8 * g + 32 * (c >> 1) + 4 * al;
    v2u xt0, xt1;
    {
      const int cp = w >> 1;
      const GAS char* xs = (const GAS char*)p.ws + OFF_XR + (2 * cj + (cp >> 1)) * (S * 2) + 2 * (1 - (cp & 1)) + (w & 1) * 2048 + lane * 16;
      dma2s(xs, LB + B_XC + cp * B_XCS + (w & 1) * 2048);
    }
    asm volatile("global_load_dwordx2 %0, %1, off sc1" : "=v"(xt0) : "v"((const GAS char*)p.ws + OFF_XT + (bch * S + sa) * 2) : "memory");
    asm volatile("global_load_dwordx2 %0, %1, off offset:2048 sc1" : "=v"(xt1) : "v"((const GAS char*)p.ws + OFF_XT + (bch * S + sa) * 2) : "memory");
    dma2s((const GAS char*)p.ws + OFF_ST + w * 2048 + lane * 16, LB + B_ST + w * 2048);
    const float dch = bflo(dv);
    if (lane < 32) *(LAS u16*)(L + B_HC + w * B_HCS + 2 * lane) = ((lane & 31) + 2 * q4 - 30) >= 0 ? (u16)hval : (u16)0;
    WAIT_V(4);
    if ((w & 3) == 1 && lane == 0) *(LAS u16*)(L + B_XC + (w >> 1) * B_XCS + 2 * (S - 1)) = 0;
    BARRIER();
    STAMPT(5);

    LAS unsigned char* ab = L + B_HC + (chl * 4 + (c & 3)) * B_HCS + 2 * (224 + 8 * (c >> 2) + 8 * g) - 128 * q4;
    LAS unsigned char* bb = L + B_XC + (chl * 2 + al) * B_XCS + 2 * (2240 - 1792 - 32 * (c >> 1) + 8 * g) - 128 * q4;
    LAS f32x4* red = (LAS f32x4*)(L + B_RED);
    const int rw = w;
    auto bfinish = [&](int hf, v2u xt) {
      LAS f32x4* rp = red + (chl * 32 + hf * 4 + q4) * 64 + lane;
      const f32x4 a0 = rp[0], a1 = rp[8 * 64], a2 = rp[16 * 64], a3 = rp[24 * 64];
      const int s = sa + 1024 * hf;
      const v2u kvr = *(LAS v2u*)(L + B_XC + (chl * 2 + 1) * B_XCS + 2 * (S - 4 - s));
      v4u g0 = *(LAS v4u*)(L + B_ST + 8 * s), g1 = *(LAS v4u*)(L + B_ST + 8 * s + 16);
      if (__builtin_amdgcn_ballot_w64(g0.y == tag && g0.w == tag && g1.y == tag && g1.w == tag) != ~0ull) {
        for (int spins = 0; spins < SPIN_CAP; ++spins) {
          unsigned long long e[4];
#pragma unroll
          for (int i = 0; i < 4; ++i) e[i] = __hip_atomic_load((const GAS unsigned long long*)(p.ws + OFF_ST + 8 * s) + i, __ATOMIC_RELAXED, __HIP_MEMORY_SCOPE_AGENT);
          g0 = v4u{(unsigned)e[0], (unsigned)(e[0] >> 32), (unsigned)e[1], (unsigned)(e[1] >> 32)};
          g1 = v4u{(unsigned)e[2], (unsigned)(e[2] >> 32), (unsigned)e[3], (unsigned)(e[3] >> 32)};
          if (__builtin_amdgcn_ballot_w64(g0.y == tag && g0.w == tag && g1.y == tag && g1.w == tag) == ~0ull) break;
          __builtin_amdgcn_s_sleep(2);
        }
      }
      const unsigned u0 = g0.x, u1 = g0.z, u2 = g1.x, u3 = g1.z;
      const f32x2 mr01 = {bflo(u0), bflo(u1)}, rr01 = {bfhi(u0), bfhi(u1)}, mr23 = {bflo(u2), bflo(u3)}, rr23 = {bfhi(u2), bfhi(u3)};
      const f32x2 ylo = (a0.xy + a1.xy) + (a2.xy + a3.xy), yhi = (a0.zw + a1.zw) + (a2.zw + a3.zw);
      const float s0_ = al ? ylo.x : yhi.x, s1_ = al ? ylo.y : yhi.y;
      const float r0_ = asf(__builtin_amdgcn_update_dpp(0, asu(s0_), 0xB1, 0xf, 0xf, true));
      const float r1_ = asf(__builtin_amdgcn_update_dpp(0, asu(s1_), 0xB1, 0xf, 0xf, true));
      const f32x2 y01 = {al ? r0_ : ylo.x, al ? yhi.x : r0_}, y23 = {al ? r1_ : ylo.y, al ? yhi.y : r1_};
      const unsigned k1 = kvr.y, k0 = kvr.x;
      const f32x2 x01 = up2(xt.x), x23 = up2(xt.y);
      const f32x2 dd = {dch, dch};
      const f32x2 t01 = fma2(f32x2{bfhi(k1), bflo(k1)}, dd, y01), t23 = fma2(f32x2{bfhi(k0), bflo(k0)}, dd, y23);
      const f32x2 h01 = fma2(fma2(x01, rr01, mr01), t01, x01), h23 = fma2(fma2(x23, rr23, mr23), t23, x23);
      const int sl = (s & 63) >> 2;
      __builtin_amdgcn_raw_buffer_store_b64(v2u{pk(h01.x, h01.y), pk(h23.x, h23.y)}, rs,
                                            OFF_HT + ((s >> 6) * D + bch) * 128 + ((sl ^ (((bch >> 1) & 1) << 3)) << 3), 0, SC1);
    };
    {
      v4u af[8], bf[8], ag[8], bg[8];
      f32x4 acc[4];
#pragma unroll
      for (int i = 0; i < 4; ++i) acc[i] = f32x4{0.f, 0.f, 0.f, 0.f};
#pragma unroll
      for (int i = 0; i < 4; ++i) { bf[i] = *(LAS v4u*)(bb + 512 * (7 - i)); af[i] = *(LAS v4u*)(ab + 512 * i); }
#pragma unroll
      for (int i = 0; i < 4; ++i) { bg[i] = *(LAS v4u*)(bb - 64 + 512 * (7 - i)); ag[i] = *(LAS v4u*)(ab - 64 + 512 * i); }
      SCHED();
#pragma unroll
      for (int ip = 0; ip < 4; ++ip)
#pragma unroll
        for (int i = ip; i < 4; ++i) acc[i] = mfma16(af[i - ip], bf[ip], acc[i]);
#pragma unroll
      for (int i = 4; i < 8; ++i) { bf[i] = *(LAS v4u*)(bb + 512 * (7 - i)); af[i] = *(LAS v4u*)(ab + 512 * i); }
      SCHED();

#pragma unroll
      for (int i = 0; i < 4; ++i)
#pragma unroll
        for (int ip = 0; ip <= i; ++ip) acc[i] = mfma16(ag[i - ip], bg[ip], acc[i]);
      SCHED();
      asm volatile("s_waitcnt vmcnt(0)" : "+v"(xt0), "+v"(xt1) :: "memory");
      if (w >= 4 && bid < 224) {
#pragma unroll
        for (int cc = 0; cc < 2; ++cc) {
          const int v = w & 3;
          const int off = v * 2048 + (16 * cc + 8 * hh + tq) * 64 + 32 * blk + 16 * (tp & 1) + 8 * (tp >> 1);
          const v4u f1 = trfrag(L + A_W1 + off, 256);
          const v4u f2 = trfrag(L + A_W2 + off, 256);
          const int k16 = 2 * (bid / 14) + cc, nt = bid % 14;
          st16(f1, rs, OFF_W1 + (((nt * 8 + (k16 >> 4) * 4 + v) * 16) + (k16 & 15)) * 1024 + lane * 16);
          const int ka = 2 * (bid >> 2) + cc, dt = 2 * (bid & 3) + (v >> 1);
          st16(f2, rs, OFF_W2 + (((dt * 8 + ka / 14) * 28) + (ka % 14) * 2 + (v & 1)) * 1024 + lane * 16);
        }
      }
#pragma unroll
      for (int i = 0; i < 4; ++i) red[(rw * 8 + i) * 64 + lane] = acc[i];
      BARRIER();
      STAMPT(21);
      f32x4 ac2[4];
#pragma unroll
      for (int i = 0; i < 4; ++i) ac2[i] = f32x4{0.f, 0.f, 0.f, 0.f};
      bfinish(0, xt0);
      STAMPT(26);
      SCHED();
#pragma unroll
      for (int ip = 0; ip < 8; ++ip) {
#pragma unroll
        for (int i = (ip > 4 ? ip : 4); i < 8; ++i) ac2[i - 4] = mfma16(af[i - ip], bf[ip], ac2[i - 4]);
        SCHED();
        if (ip < 4) { ag[4 + ip] = *(LAS v4u*)(ab - 64 + 512 * (4 + ip)); bg[4 + ip] = *(LAS v4u*)(bb - 64 + 512 * (7 - 4 - ip)); }
        SCHED();
      }
      WAIT_V(0);
      lds_count_flag8(L, 4, lane, tag, rs, OFF_FB0 + bid * 16);
      asm volatile("global_load_ushort %0, %1, %2" : "=v"(b1n) : "v"((unsigned)(128 * d_nt + 32 * nq + (lane & 31)) * 2), "s"(p.b1) : "memory");
      SCHED();
#pragma unroll
      for (int i = 4; i < 8; ++i) {
#pragma unroll
        for (int ip = 0; ip <= i; ++ip) {
          ac2[i - 4] = mfma16(ag[i - ip], bg[ip], ac2[i - 4]);
        }
        SCHED();
        if (i > 4) red[(rw * 8 + 4 + i - 5) * 64 + lane] = ac2[i - 5];
        SCHED();
      }
      red[(rw * 8 + 7) * 64 + lane] = ac2[3];
    }
    BARRIER();
    STAMPT(6);
    if (w == 0) LOOK_ISSUE(OFF_FB0 + xcd * 4096 + lane * 16);
    bfinish(1, xt1);
    STAMPT(7);
    if (w == 0) LOOK_CHECK(1, OFF_FB0 + xcd * 4096 + lane * 16);
    STAMPT(8);
  }

  {
    BARRIER();
    STAMPT(9);
    const GAS unsigned char* tsrc = (const GAS unsigned char*)p.ws + OFF_HT + (size_t)d_mt * 65536 + w * 8192 + lane * 16;
    const unsigned w2off = OFF_W2 + (e_dt * 8 + w) * 28 * 1024 + lane * 16;
    auto second_wait = [&]() {
      if (w == 0) { poll4(rs, OFF_FB1 + xcd * 4096 + lane * 16, tag); lds_set(L, 0); }
      lds_wait(L, 0);
    };
    auto second_res = [&]() {
      const int rr = 16 * (w & 3) + (lane >> 2);
      const int dch2 = 64 * e_dt + rr;
      dma1s((const GAS unsigned char*)p.ws + OFF_HT + ((16 * (w >> 2) + e_mt) * D + dch2) * 128 + (((unsigned)e_j ^ ((dch2 >> 1) & 1)) << 6) + 16 * (lane & 3),
            LB + E_RES + w * 1024);
    };
    if (d_act) { dma4s(tsrc, LB + D_TILE + w * 8192); dma4s(tsrc + 4096, LB + D_TILE + w * 8192 + 4096); }
    if (d_act) { WAIT_V(8); } else { WAIT_V(0); }
    lds_count_flag8(L, 5, lane, tag, rs, OFF_FB1 + bid * 16);
    if (d_act) {
      asm volatile("" : "+v"(b1n) :: "memory");
#pragma unroll
      for (int i = 0; i < 8; ++i) w1load(i);
      SCHED();
      const unsigned basel = (unsigned)(D_TILE + (256 * kh + 8 * hh + tq) * 128 + 32 * blk + 8 * tp);
      const unsigned fsw = ((tq >> 1) & 1) << 6;
      const unsigned adr[2] = {basel + ((unsigned)(kh << 6) ^ fsw), basel + ((unsigned)((kh ^ 1) << 6) ^ fsw)};
      f32x16 acc[2][2];
      auto fraddr = [&](int c, int m) { return L + (adr[m & 1] + (unsigned)(c * 65536 + 8192 * ((((m >> 1) >> 2) + nq) & 3)) + 2048 * ((m >> 1) & 3)); };
      float xg[16];
      unsigned og[8];
      auto hstore = [&](int c) {
        const unsigned ho = OFF_HF + ((32 * c + 2 * d_mt + kh) * 112 + 8 * d_nt + 2 * nq) * 1024 + lane * 16;
        st16(v4u{og[0], og[1], og[2], og[3]}, rs, ho);
        st16(v4u{og[4], og[5], og[6], og[7]}, rs, ho + 1024);
      };
      v2u sg[2][2];
      auto sload = [&](int c) {
        const GAS char* sp = (const GAS char*)p.ws + OFF_HS + (1024 * c + 64 * d_mt + 32 * kh + (lane & 31)) * 16;
        asm volatile("global_load_dwordx2 %0, %1, off sc1" : "=v"(sg[c][0]) : "v"(sp) : "memory");
        asm volatile("global_load_dwordx2 %0, %1, off offset:8 sc1" : "=v"(sg[c][1]) : "v"(sp) : "memory");
      };
      auto mstep = [&](int c) {
#pragma unroll
        for (int r = 0; r < 2; ++r)
#pragma unroll
          for (int e = 0; e < 16; ++e) acc[c][r][e] = 0.f;
        v4u fr[4];
#pragma unroll
        for (int n = 0; n < 3; ++n) fr[n] = trfrag(fraddr(c, n), 512);
        SCHED();
#pragma unroll
        for (int n = 0; n < 32; ++n) {
          const int i = n >> 1, r = n & 1;
          if (c == 0 && r == 0) {
            if (n < 16) { w1load(8 + i); WAITF(8, wf1[i]); }
            if (n == 16) {
              WAITF(7, wf1[8]);
              if (w == 0) { LOOK_CHECK(4, OFF_FB1 + xcd * 4096 + lane * 16); lds_set(L, 0); }
              lds_wait(L, 0);
            }
            if (n == 18) WAITF(7, wf1[9]);
            if (n == 20) WAITF(7, wf1[10]);
            if (n == 22) WAITF(7, wf1[11]);
            if (n == 24) WAITF(7, wf1[12]);
            if (n == 26) WAITF(7, wf1[13]);
            if (n == 28) WAITF(7, wf1[14]);
            if (n == 30) WAITF(9, wf1[15]);
          }
          if (c == 0 && n == 7 && w == 0) LOOK_ISSUE(OFF_FB1 + xcd * 4096 + lane * 16);
          if (c == 0 && n >= 16 && (n & 1) == 0) dma1s(tsrc + 16 * 65536 + ((n - 16) >> 1) * 1024, LB + D_TILE + 65536 + w * 8192 + ((n - 16) >> 1) * 1024);
          if (c == 1 && n == 1) second_res();
          if (c == 1 && n == 15) sload(1);
          if (c == 0 && n == 29) {
            sload(0);
            if (w == 0) dma1s((const GAS char*)p.ws + OFF_CS + d_nt * 1024 + lane * 16, LB + D_CSG);
          }
          if (n + 3 < 32) fr[(n + 3) & 3] = trfrag(fraddr(c, n + 3), 512);
          acc[c][r] = mfma32(wf1[i], fr[n & 3], acc[c][r]);
          if (c == 1 && (n & 1) == 0) wf2[n >> 1] = ld16(rs, w2off + (n >> 1) * 1024);
          SCHED();
        }
      };
      auto xwrite = [&](int c) {
        LAS f32x4* ex = (LAS f32x4*)(L + D_TILE + c * 32768) + w * 256 + lane;
#pragma unroll
        for (int qd = 0; qd < 4; ++qd) ex[qd * 64] = f32x4{acc[c][1][4 * qd], acc[c][1][4 * qd + 1], acc[c][1][4 * qd + 2], acc[c][1][4 * qd + 3]};
      };
      auto xpre = [&](int c) {
        LAS f32x4* ex = (LAS f32x4*)(L + D_TILE + c * 32768) + (w ^ 4) * 256 + lane;
        LAS f32x4* cb = (LAS f32x4*)(L + D_CB) + 16 * nq + 4 * hh;
        f32x4 pp[4], c0[4], c1[4];
#pragma unroll
        for (int qd = 0; qd < 4; ++qd) { pp[qd] = ex[qd * 64]; c0[qd] = cb[8 * (qd >> 1) + 2 * (qd & 1)]; c1[qd] = cb[8 * (qd >> 1) + 2 * (qd & 1) + 1]; }
        SCHED();
        unsigned u0 = sg[c][0].x, u1 = sg[c][1].x;
        if (__builtin_amdgcn_ballot_w64(((sg[c][0].y ^ tag) | (sg[c][1].y ^ tag)) != 0u) != 0ull) {
          const GAS unsigned long long* sp = (const GAS unsigned long long*)(p.ws + OFF_HS + (1024 * c + 64 * d_mt + 32 * kh + (lane & 31)) * 16);
          for (int spins = 0; spins < SPIN_CAP; ++spins) {
            const unsigned long long e0 = __hip_atomic_load(sp, __ATOMIC_RELAXED, __HIP_MEMORY_SCOPE_AGENT), e1 = __hip_atomic_load(sp + 1, __ATOMIC_RELAXED, __HIP_MEMORY_SCOPE_AGENT);
            u0 = (unsigned)e0; u1 = (unsigned)e1;
            if (__builtin_amdgcn_ballot_w64((((unsigned)(e0 >> 32) ^ tag) | ((unsigned)(e1 >> 32) ^ tag)) != 0u) == 0ull) break;
            __builtin_amdgcn_s_sleep(2);
          }
        }
        const float rstd = asf(u0), nm = asf(u1);
        const f32x2 rs2 = {rstd, rstd}, nm2 = {nm, nm};
#pragma unroll
        for (int qd = 0; qd < 4; ++qd)
#pragma unroll
          for (int hf = 0; hf < 2; ++hf) {
            const int e = 2 * qd + hf;
            const f32x4 cc = hf ? c1[qd] : c0[qd];
            const f32x2 a = f32x2{acc[c][0][2 * e], acc[c][0][2 * e + 1]} + (hf ? pp[qd].zw : pp[qd].xy);
            const f32x2 x = fma2(rs2, a, fma2(nm2, cc.xz, cc.yw));
            xg[2 * e] = x.x; xg[2 * e + 1] = x.y;
          }
      };
      auto cbtable = [&]() {
        const int n = 32 * nq + (lane & 31);
        v2u gq = *(LAS v2u*)(L + D_CSG + 8 * n);
        unsigned cs = gq.x;
        if (__builtin_amdgcn_ballot_w64(gq.y != tag) != 0ull) {
          for (int spins = 0; spins < SPIN_CAP; ++spins) {
            const unsigned long long e0 = __hip_atomic_load((const GAS unsigned long long*)(p.ws + OFF_CS) + 128 * d_nt + n, __ATOMIC_RELAXED, __HIP_MEMORY_SCOPE_AGENT);
            cs = (unsigned)e0;
            if (__builtin_amdgcn_ballot_w64((unsigned)(e0 >> 32) != tag) == 0ull) break;
            __builtin_amdgcn_s_sleep(2);
          }
        }
        *(LAS f32x2*)(L + D_CB + 8 * n) = f32x2{asf(cs), bflo(b1n)};
        asm volatile("s_waitcnt lgkmcnt(0)" ::: "memory");
      };
      auto gelu_all = [&]() {
#pragma unroll
        for (int e = 0; e < 8; ++e) {
          const f32x2 x = {xg[2 * e], xg[2 * e + 1]};
          const f32x2 pz = fma2(x * x, f32x2{-0.10294324f, -0.10294324f}, f32x2{-2.3022082f, -2.3022082f});
          const f32x2 z = x * pz;
          const f32x2 den = f32x2{__builtin_amdgcn_exp2f(z.x), __builtin_amdgcn_exp2f(z.y)} + f32x2{1.f, 1.f};
          const f32x2 y = x * f32x2{__builtin_amdgcn_rcpf(den.x), __builtin_amdgcn_rcpf(den.y)};
          og[e] = pk(y.x, y.y);
        }
      };
      WAIT_V(8);
      BARRIER();
      STAMPT(10);
      SCHED();
      mstep(0);
      STAMPT(22);
      BARRIER();
      STAMPT(11);
      xwrite(0);
      asm volatile("s_waitcnt vmcnt(0)" : "+v"(sg[0][0]), "+v"(sg[0][1]) :: "memory");
      BARRIER();
      STAMPT(23);
      cbtable();
      xpre(0);
      gelu_all();
      hstore(0);
      BARRIER();
      STAMPT(24);
      mstep(1);
      STAMPT(25);
      xwrite(1);
      asm volatile("s_waitcnt vmcnt(8)" : "+v"(sg[1][0]), "+v"(sg[1][1]) :: "memory");
      lds_count_flag(L, 2, 8, tid, d_act, tag, rs, OFF_FD + d_mt * 256 + d_nt * 16);
      BARRIER();
      STAMPT(12);
      xpre(1);
      gelu_all();
      hstore(1);
      STAMPT(13);
    }
    asm volatile("" ::: "memory");
    if (!d_act) {
      const int u = xcd * 4 + (bix - 28);
      if (u >= 16) {
        const int kg = tid >> 4, cc = tid & 15;
        f32x2 cs[4] = {{0.f, 0.f}, {0.f, 0.f}, {0.f, 0.f}, {0.f, 0.f}};
        if (cc < 14) {
          v4u wv[16];
#pragma unroll
          for (int i = 0; i < 16; ++i) wv[i] = *(const GAS v4u*)((const GAS char*)p.w1 + (unsigned)((16 * kg + i) * F + 112 * (u - 16) + 8 * cc) * 2);
#pragma unroll
          for (int i = 0; i < 16; ++i) { cs[0] += up2(wv[i].x); cs[1] += up2(wv[i].y); cs[2] += up2(wv[i].z); cs[3] += up2(wv[i].w); }
        }
        LAS f32x4* t = (LAS f32x4*)(L + 65536) + (kg * 16 + cc) * 2;
        t[0] = f32x4{cs[0].x, cs[0].y, cs[1].x, cs[1].y}; t[1] = f32x4{cs[2].x, cs[2].y, cs[3].x, cs[3].y};
        BARRIER();
        if (tid < 112) {
          float a = 0.f;
#pragma unroll
          for (int i = 0; i < 32; ++i) a += *(LAS float*)(L + 65536 + (i * 128 + tid) * 4);
          __builtin_amdgcn_raw_buffer_store_b64(v2u{asu(a), tag}, rs, OFF_CS + 8 * (112 * (u - 16) + tid), 0, SC1);
        }
      }
      if (u >= 16) second_wait();
      {
        const GAS unsigned char* src = (const GAS unsigned char*)p.ws + OFF_HT + (size_t)u * 65536 + w * 8192 + lane * 16;
        dma4s(src, LB + w * 8192); dma4s(src + 4096, LB + w * 8192 + 4096);
      }
      WAIT_V(0);
      {
        float s1[2][2] = {{0.f, 0.f}, {0.f, 0.f}}, s2[2][2] = {{0.f, 0.f}, {0.f, 0.f}};
        LAS unsigned char* sp = L + w * 8192 + (8 * hh + tq) * 128 + 32 * blk + 8 * tp;
        const unsigned fsw = ((tq >> 1) & 1) << 6;
#pragma unroll
        for (int n = 0; n < 8; ++n) {
          const int r = n & 1;
          const v4u hf = trfrag(sp + (n >> 1) * 2048 + (((unsigned)r << 6) ^ fsw), 512);
          s1[r][0] = dot2(hf.x, ONES, s1[r][0]); s2[r][0] = dot2(hf.x, hf.x, s2[r][0]);
          s1[r][1] = dot2(hf.y, ONES, s1[r][1]); s2[r][1] = dot2(hf.y, hf.y, s2[r][1]);
          s1[r][0] = dot2(hf.z, ONES, s1[r][0]); s2[r][0] = dot2(hf.z, hf.z, s2[r][0]);
          s1[r][1] = dot2(hf.w, ONES, s1[r][1]); s2[r][1] = dot2(hf.w, hf.w, s2[r][1]);
        }
#pragma unroll
        for (int r = 0; r < 2; ++r) {
          const float a = halfsum(s1[r][0] + s1[r][1]), b = halfsum(s2[r][0] + s2[r][1]);
          if (lane < 32) *(LAS f32x2*)(L + 98304 + (w * 64 + 32 * r + lane) * 8) = f32x2{a, b};
        }
      }
      BARRIER();
      if (w == 0) {
        f32x2 sa = {0.f, 0.f};
#pragma unroll
        for (int ww = 0; ww < 8; ++ww) sa += *(LAS f32x2*)(L + 98304 + (ww * 64 + lane) * 8);
        const float mean = sa.x * (1.0f / D);
        float var = __builtin_fmaf(-mean, mean, sa.y * (1.0f / D));
        var = var > 0.f ? var : 0.f;
        const float r1 = __builtin_amdgcn_rsqf(var + 1e-5f);
        const float rstd = r1 * __builtin_amdgcn_rsqf(__builtin_fmaf(var * r1, r1, 1e-5f));
        __builtin_amdgcn_raw_buffer_store_b64(v2u{asu(rstd), tag}, rs, OFF_HS + (64 * u + lane) * 16, 0, SC1);
        __builtin_amdgcn_raw_buffer_store_b64(v2u{asu(-rstd * mean), tag}, rs, OFF_HS + (64 * u + lane) * 16 + 8, 0, SC1);
      }
      if (u < 16) second_wait();
      second_res();
#pragma unroll
      for (int i = 0; i < 16; ++i) wf2[i] = ld16(rs, w2off + i * 1024);
    }
  }

  {
    const int sbo = w >> 2, dbo = (w >> 1) & 1, hf = w & 1;
    if (w == 0) poll1(rs, OFF_FD + e_mt * 256 + (lane < 14 ? lane : 0) * 16, tag);
    const unsigned w2off = OFF_W2 + (e_dt * 8 + w) * 28 * 1024 + lane * 16;
#pragma unroll
    for (int i = 16; i < 28; ++i) wf2[i] = ld16(rs, w2off + i * 1024);
    const v4u b2v = *(const GAS v4u*)((const GAS char*)p.b2 + (unsigned)(64 * e_dt + 32 * dbo + 16 * hf + 8 * hh) * 2);
    SCHED();
    WAIT_V(13);
    BARRIER();
    if (d_act && tid == 0) st4(tag, rs, OFF_FD + 4096 + d_mt * 256 + d_nt * 16);
    STAMPT(14);
    const unsigned hoff0 = OFF_HF + ((2 * e_mt + e_j) * 112 + 14 * w) * 1024 + lane * 16, hoff1 = hoff0 + 32 * 112 * 1024;
    if (w == 0) { poll1(rs, OFF_FD + 4096 + e_mt * 256 + (lane < 14 ? lane : 0) * 16, tag); lds_set(L, 1); }
    v4u hf0[14], hf1[14];
#pragma unroll
    for (int k = 0; k < 14; ++k) hf0[k] = ld16(rs, hoff0 + k * 1024);
    SCHED();
    lds_wait(L, 1);
    STAMPT(15);
    LAS f32x4* red = (LAS f32x4*)(L + E_RED + w * 16384) + lane;
    {
      f32x16 acc[2];
#pragma unroll
      for (int e = 0; e < 16; ++e) { acc[0][e] = 0.f; acc[1][e] = 0.f; }
#pragma unroll
      for (int k = 0; k < 14; ++k) {
        acc[0] = mfma32(wf2[2 * k], hf0[k], acc[0]); acc[1] = mfma32(wf2[2 * k + 1], hf0[k], acc[1]);
        SCHED();
        hf1[k] = ld16(rs, hoff1 + k * 1024);
        SCHED();
      }
#pragma unroll
      for (int d = 0; d < 2; ++d)
#pragma unroll
        for (int qd = 0; qd < 4; ++qd) red[(d * 4 + qd) * 64] = f32x4{acc[d][4 * qd], acc[d][4 * qd + 1], acc[d][4 * qd + 2], acc[d][4 * qd + 3]};
    }
    STAMPT(16);
    {
      f32x16 acc[2];
#pragma unroll
      for (int e = 0; e < 16; ++e) { acc[0][e] = 0.f; acc[1][e] = 0.f; }
#pragma unroll
      for (int k = 0; k < 14; ++k) { acc[0] = mfma32(wf2[2 * k], hf1[k], acc[0]); acc[1] = mfma32(wf2[2 * k + 1], hf1[k], acc[1]); }
#pragma unroll
      for (int d = 0; d < 2; ++d)
#pragma unroll
        for (int qd = 0; qd < 4; ++qd) red[512 + (d * 4 + qd) * 64] = f32x4{acc[d][4 * qd], acc[d][4 * qd + 1], acc[d][4 * qd + 2], acc[d][4 * qd + 3]};
    }
    BARRIER();
    STAMPT(17);
    f32x2 o[4] = {{0.f, 0.f}, {0.f, 0.f}, {0.f, 0.f}, {0.f, 0.f}};
#pragma unroll
    for (int ww = 0; ww < 8; ++ww) {
      LAS f32x4* rp = (LAS f32x4*)(L + E_RED + ww * 16384) + sbo * 512 + (dbo * 4 + 2 * hf) * 64 + lane;
      const f32x4 a = rp[0], b = rp[64];
      o[0] += a.xy; o[1] += a.zw; o[2] += b.xy; o[3] += b.zw;
    }
    const v4u hr = trfrag(L + E_RES + sbo * 4096 + (32 * dbo + 16 * hf + 8 * hh + tq) * 64 + 32 * blk + 8 * tp, 256);
    o[0] += up2(b2v.x) + up2(hr.x); o[1] += up2(b2v.y) + up2(hr.y);
    o[2] += up2(b2v.z) + up2(hr.z); o[3] += up2(b2v.w) + up2(hr.w);
    const v4u ov = {pk(o[0].x, o[0].y), pk(o[1].x, o[1].y), pk(o[2].x, o[2].y), pk(o[3].x, o[3].y)};
    {
      const rsrc_t ro = __builtin_amdgcn_make_buffer_rsrc((void*)p.out, 0, S * D * 2, 0x00020000);
      st16(ov, ro, (unsigned)((1024 * sbo + 64 * e_mt + 32 * e_j + (lane & 31)) * D + 64 * e_dt + 32 * dbo + 16 * hf + 8 * hh) * 2);
    }
    if (bid == 0 && tid == 0) st4(tag, rs, OFF_EP);
    STAMPT(18);
  }
}

extern "C" void kernel_launch(void* const* d_in, const int* in_sizes, int n_in, void* d_out, int out_size, void* d_ws,
                              size_t ws_size, hipStream_t stream) {
  Params p;
  p.q = (const u16*)d_in[0]; p.k = (const u16*)d_in[1]; p.v = (const u16*)d_in[2];
  p.shk = (const u16*)d_in[3]; p.ssk = (const u16*)d_in[4]; p.ssd = (const u16*)d_in[5];
  p.w1 = (const u16*)d_in[7]; p.b1 = (const u16*)d_in[8]; p.w2 = (const u16*)d_in[9]; p.b2 = (const u16*)d_in[10];
  p.out = (u16*)d_out; p.ws = (unsigned char*)d_ws;
  fused<<<256, 512, 0, stream>>>(p);
}
```

```cpp
#include <hip/hip_runtime.h>
#include <hip/hip_bf16.h>

#define GAS __attribute__((address_space(1)))
#define LAS __attribute__((address_space(3)))
typedef unsigned short u16;
typedef unsigned v4u __attribute__((ext_vector_type(4)));
typedef unsigned v2u __attribute__((ext_vector_type(2)));
typedef float f32x16 __attribute__((ext_vector_type(16)));
typedef float f32x4 __attribute__((ext_vector_type(4)));
typedef float f32x2 __attribute__((ext_vector_type(2)));
typedef short s16x4 __attribute__((ext_vector_type(4)));
typedef short bf16x8 __attribute__((ext_vector_type(8)));
typedef __bf16 bf2 __attribute__((ext_vector_type(2)));
typedef __amdgpu_buffer_rsrc_t rsrc_t;

constexpr int S = 2048, D = 512, F = 1792;
constexpr unsigned OFF_EP = 0;
constexpr unsigned OFF_FA1 = 4096;
constexpr unsigned OFF_FD = 8192;
constexpr unsigned OFF_FB0 = 16384;
constexpr unsigned OFF_FB1 = OFF_FB0 + 32768;
constexpr unsigned OFF_XR = 262144;
constexpr unsigned OFF_XT = OFF_XR + D * S * 2;
constexpr unsigned OFF_HT = OFF_XT + D * S * 2;
constexpr unsigned OFF_ST = OFF_HT + D * S * 2;
constexpr unsigned OFF_HS = OFF_ST + 16384;
constexpr unsigned OFF_CS = OFF_HS + 32768;
constexpr unsigned OFF_W1 = OFF_CS + 16384;
constexpr unsigned OFF_W2 = OFF_W1 + D * F * 2;
constexpr unsigned OFF_HF = OFF_W2 + D * F * 2;
constexpr unsigned WS_END = OFF_HF + S * F * 2;
constexpr int SPIN_CAP = 1 << 16;

struct Params {
  const u16 *q, *k, *v, *shk, *ssk, *ssd, *w1, *b1, *w2, *b2;
  u16* out;
  unsigned char* ws;
};

__device__ __forceinline__ float asf(unsigned u) { return __builtin_bit_cast(float, u); }
__device__ __forceinline__ unsigned asu(float f) { return __builtin_bit_cast(unsigned, f); }
__device__ __forceinline__ float bflo(unsigned u) { return asf(u << 16); }
__device__ __forceinline__ float bfhi(unsigned u) { return asf(u & 0xffff0000u); }
__device__ __forceinline__ unsigned pk(float lo, float hi) {
  f32x2 f = {lo, hi};
  return __builtin_bit_cast(unsigned, __builtin_convertvector(f, bf2));
}
__device__ __forceinline__ f32x2 up2(unsigned u) { return f32x2{bflo(u), bfhi(u)}; }
__device__ __forceinline__ f32x2 fma2(f32x2 a, f32x2 b, f32x2 c) { return __builtin_elementwise_fma(a, b, c); }
__device__ __forceinline__ f32x4 mfma16(v4u a, v4u b, f32x4 c) {
  return __builtin_amdgcn_mfma_f32_16x16x32_bf16(__builtin_bit_cast(bf16x8, a), __builtin_bit_cast(bf16x8, b), c, 0, 0, 0);
}
__device__ __forceinline__ f32x16 mfma32(v4u a, v4u b, f32x16 c) {
  return __builtin_amdgcn_mfma_f32_32x32x16_bf16(__builtin_bit_cast(bf16x8, a), __builtin_bit_cast(bf16x8, b), c, 0, 0, 0);
}
__device__ __forceinline__ v2u trread(LAS unsigned char* p) {
  return __builtin_bit_cast(v2u, __builtin_amdgcn_ds_read_tr16_b64_v4i16((LAS s16x4*)p));
}
__device__ __forceinline__ v4u trfrag(LAS unsigned char* p, int second) {
  const v2u a = trread(p), b = trread(p + second);
  return v4u{a.x, a.y, b.x, b.y};
}
__device__ __forceinline__ float dot2(unsigned a, unsigned b, float c) {
  return __builtin_amdgcn_fdot2_f32_bf16(__builtin_bit_cast(bf2, a), __builtin_bit_cast(bf2, b), c, false);
}
constexpr int SC1 = 16;
#define DMA_HEAD "s_mov_b32 m0, %1\n\ts_nop 0\n\t"
#define DMA_P(o, f) "global_load_lds_dwordx4 %0, off offset:" #o f "\n\t"
__device__ __forceinline__ void dma4(const GAS void* g, unsigned lds) {
  asm volatile(DMA_HEAD DMA_P(0, "") DMA_P(1024, "") DMA_P(2048, "") DMA_P(3072, "") :: "v"(g), "s"(lds) : "memory");
}
__device__ __forceinline__ void dma3(const GAS void* g, unsigned lds) {
  asm volatile(DMA_HEAD DMA_P(0, "") DMA_P(1024, "") DMA_P(2048, "") :: "v"(g), "s"(lds) : "memory");
}
__device__ __forceinline__ void dma1(const GAS void* g, unsigned lds) {
  asm volatile(DMA_HEAD DMA_P(0, "") :: "v"(g), "s"(lds) : "memory");
}
__device__ __forceinline__ void dma4s(const GAS void* g, unsigned lds) {
  asm volatile(DMA_HEAD DMA_P(0, " sc1") DMA_P(1024, " sc1") DMA_P(2048, " sc1") DMA_P(3072, " sc1") :: "v"(g), "s"(lds) : "memory");
}
__device__ __forceinline__ void dma3s(const GAS void* g, unsigned lds) {
  asm volatile(DMA_HEAD DMA_P(0, " sc1") DMA_P(1024, " sc1") DMA_P(2048, " sc1") :: "v"(g), "s"(lds) : "memory");
}
__device__ __forceinline__ void dma2s(const GAS void* g, unsigned lds) {
  asm volatile(DMA_HEAD DMA_P(0, " sc1") DMA_P(1024, " sc1") :: "v"(g), "s"(lds) : "memory");
}
__device__ __forceinline__ void dma1s(const GAS void* g, unsigned lds) {
  asm volatile(DMA_HEAD DMA_P(0, " sc1") :: "v"(g), "s"(lds) : "memory");
}
__device__ __forceinline__ v4u ld16(rsrc_t rs, unsigned off) { return __builtin_amdgcn_raw_buffer_load_b128(rs, off, 0, SC1); }
__device__ __forceinline__ void st16(v4u v, rsrc_t rs, unsigned off) { __builtin_amdgcn_raw_buffer_store_b128(v, rs, off, 0, SC1); }
__device__ __forceinline__ void st4(unsigned v, rsrc_t rs, unsigned off) { __builtin_amdgcn_raw_buffer_store_b32(v, rs, off, 0, SC1); }
template <int CTRL, int RM>
__device__ __forceinline__ float dppadd(float x) {
  return x + asf(__builtin_amdgcn_update_dpp(0, asu(x), CTRL, RM, 0xf, true));
}
__device__ __forceinline__ float wavesum(float x) {
  x = dppadd<0x111, 0xf>(x); x = dppadd<0x112, 0xf>(x); x = dppadd<0x114, 0xf>(x); x = dppadd<0x118, 0xf>(x);
  x = dppadd<0x142, 0xa>(x); x = dppadd<0x143, 0xc>(x);
  return x;
}
__device__ __forceinline__ float halfsum(float x) {
  const unsigned u = asu(x);
  auto r = __builtin_amdgcn_permlane32_swap(u, u, false, false);
  const unsigned r0 = r[0], r1 = r[1];
  return asf(r0) + asf(r1);
}
#define WAIT_V(n) asm volatile("s_waitcnt vmcnt(" #n ")" ::: "memory")
#define BARRIER() do { asm volatile("s_waitcnt lgkmcnt(0)" ::: "memory"); __builtin_amdgcn_s_barrier(); asm volatile("" ::: "memory"); } while (0)
#define SCHED() __builtin_amdgcn_sched_barrier(0)
constexpr unsigned ONES = 0x3f803f80u;

#define STAMPT(idx)

#define TOUCH 1
#define OUTSC1 0
#define CODEPF 0
constexpr unsigned CODE_BYTES = 22016;
constexpr int LDS_BYTES = 163840;
constexpr int A_K = 0, A_V = A_K + 68 * 128, A_Q = A_V + 8192, A_END = A_Q + 8192;
constexpr int B_HCS = 4160, B_XCS = 4640;
constexpr int L_FLAG = LDS_BYTES - 64;
constexpr int B_XC = L_FLAG - 4 * B_XCS, B_HC = B_XC - 8 * B_HCS;
constexpr int B_RED = 0, B_ST = 65536, A_W1 = B_ST + 16384, A_W2 = A_W1 + 8192;
constexpr int D_TILE = 0, D_CSG = 131072, D_CB = D_CSG + 1024, D_END = D_CB + 1024;
constexpr int E_RED = 0, E_RES = 143360;
static_assert(A_W2 + 8192 <= B_HC && A_END <= 65536 && B_HC % 16 == 0 && D_END <= E_RES && E_RES + 8192 <= L_FLAG, "");

__device__ __forceinline__ void poll4(rsrc_t rs, unsigned off, unsigned tag) {
  for (int spins = 0; spins < SPIN_CAP; ++spins) {
    const unsigned f0 = __builtin_amdgcn_raw_buffer_load_b32(rs, off, 0, SC1), f1 = __builtin_amdgcn_raw_buffer_load_b32(rs, off + 1024, 0, SC1);
    const unsigned f2 = __builtin_amdgcn_raw_buffer_load_b32(rs, off + 2048, 0, SC1), f3 = __builtin_amdgcn_raw_buffer_load_b32(rs, off + 3072, 0, SC1);
    if (__builtin_amdgcn_ballot_w64(((f0 ^ tag) | (f1 ^ tag) | (f2 ^ tag) | (f3 ^ tag)) != 0u) == 0ull) break;
    __builtin_amdgcn_s_sleep(1);
    asm volatile("" ::: "memory");
  }
}
__device__ __forceinline__ void poll1(rsrc_t rs, unsigned off, unsigned tag) {
  for (int spins = 0; spins < SPIN_CAP; ++spins) {
    const unsigned f = __builtin_amdgcn_raw_buffer_load_b32(rs, off, 0, SC1);
    if (__builtin_amdgcn_ballot_w64(f == tag) == ~0ull) break;
    __builtin_amdgcn_s_sleep(1);
    asm volatile("" ::: "memory");
  }
}
__device__ __forceinline__ void lds_set(LAS unsigned char* L, int idx) {
  asm volatile("" ::: "memory");
  *(volatile LAS unsigned*)(L + L_FLAG + 4 * idx) = 1u;
}
__device__ __forceinline__ void lds_wait(LAS unsigned char* L, int idx) {
  for (int spins = 0; spins < (SPIN_CAP << 4); ++spins) {
    if (__builtin_amdgcn_readfirstlane(*(volatile LAS unsigned*)(L + L_FLAG + 4 * idx)) != 0) break;
    __builtin_amdgcn_s_sleep(1);
  }
  asm volatile("" ::: "memory");
}
#define GLD(dst, voff, base, imm) asm volatile("global_load_dwordx4 %0, %1, %2 offset:" #imm " sc1" : "=v"(dst) : "v"(voff), "s"(base) : "memory")
__device__ __forceinline__ void lds_count_flag(LAS unsigned char* L, int idx, unsigned n, int tid, bool act, unsigned tag, rsrc_t rs, unsigned off) {
  if ((tid & 63) == 0) {
    const unsigned old = __hip_atomic_fetch_add((LAS unsigned*)(L + L_FLAG + 4 * idx), 1u, __ATOMIC_RELAXED, __HIP_MEMORY_SCOPE_WORKGROUP);
    if (old == n - 1 && act) st4(tag, rs, off);
  }
}
__device__ __forceinline__ bool lds_count_flag8(LAS unsigned char* L, int idx, int lane, unsigned tag, rsrc_t rs, unsigned off) {
  unsigned old = 0;
  if (lane == 0) old = __hip_atomic_fetch_add((LAS unsigned*)(L + L_FLAG + 4 * idx), 1u, __ATOMIC_RELAXED, __HIP_MEMORY_SCOPE_WORKGROUP);
  const bool last = __builtin_amdgcn_readfirstlane(old) == 7;
  if (last && lane < 8) st4(tag, rs, off + lane * 4096);
  return last;
}
#define LOOK_ISSUE(off) do { const GAS char* fp_ = (const GAS char*)p.ws + (off); \
    asm volatile("global_load_dword %0, %1, off sc1" : "=v"(pf0) : "v"(fp_) : "memory"); \
    asm volatile("global_load_dword %0, %1, off offset:1024 sc1" : "=v"(pf1) : "v"(fp_) : "memory"); \
    asm volatile("global_load_dword %0, %1, off offset:2048 sc1" : "=v"(pf2) : "v"(fp_) : "memory"); \
    asm volatile("global_load_dword %0, %1, off offset:3072 sc1" : "=v"(pf3) : "v"(fp_) : "memory"); } while (0)
#define LOOK_CHECK(n, off) do { asm volatile("s_waitcnt vmcnt(" #n ")" : "+v"(pf0), "+v"(pf1), "+v"(pf2), "+v"(pf3) :: "memory"); \
    if (__builtin_amdgcn_ballot_w64(pf0 == tag && pf1 == tag && pf2 == tag && pf3 == tag) != ~0ull) poll4(rs, (off), tag); } while (0)
#define WAITF(n, x) asm volatile("s_waitcnt vmcnt(" #n ")" : "+v"(x) :: "memory")
#define ALOAD(op, dst, ptr) asm volatile(op " %0, %1, off" : "=v"(dst) : "v"(ptr) : "memory")

__global__ void __launch_bounds__(512, 2) fused(Params p) {
  __shared__ __attribute__((aligned(16))) unsigned char smem[LDS_BYTES];
  LAS unsigned char* L = (LAS unsigned char*)smem;
  const unsigned LB = (unsigned)(size_t)L;
  const int tid = threadIdx.x, lane = tid & 63, w = __builtin_amdgcn_readfirstlane(tid >> 6);
  const int bid = blockIdx.x, xcd = bid & 7, bix = bid >> 3;
  const rsrc_t rs = __builtin_amdgcn_make_buffer_rsrc((void*)p.ws, 0, (int)WS_END, 0x00020000);
  const int g = lane >> 4, i16 = lane & 15, tq = i16 >> 2, tp = i16 & 3;
  const int hh = lane >> 5, blk = g & 1;
  STAMPT(0);

  const int cj = xcd * 32 + bix;
  const bool d_act = bix < 28;
  const int d_mt = 4 * (xcd >> 1) + (d_act ? bix / 7 : 0), d_nt = 7 * (xcd & 1) + bix % 7;
  const int e_mt = 2 * xcd + (bix >> 4), e_j = (bix >> 3) & 1, e_dt = bix & 7;
  const int chl = w >> 2, q4 = w & 3, bch = 2 * cj + chl;

  v4u wf1[16];
  unsigned tag;
  unsigned hval, dv;
  {
    const int b = xcd, a = bix;
    const int ch = 64 * b + 16 * (w & 3) + i16, seg = 4 * (w >> 2) + g;
    const int srow = 64 * a + 8 * b + w;
    v2u shw; v4u qs;
    unsigned dm1 = 0;
    {
      const int rl = lane >> 3, sl = lane & 7;
      const int rhok = 4 + 8 * w + rl, rhov = 8 * w + rl;
      const unsigned fk = ((rhok >> 1) & 1) | (((rhok >> 3) & 1) << 1), fv = ((rhov >> 1) & 1) | (((rhov >> 3) & 1) << 1);
      const unsigned rowoff = ((64 * a + 8 * w + rl) * D + 64 * b) * 2 + 16 * (sl & 1);
      const unsigned uk = rowoff + 32 * ((sl >> 1) ^ fk), uv = rowoff + 32 * ((sl >> 1) ^ fv);
      if (w == 0 && a > 0 && lane < 32)
        dma1((const GAS char*)p.k + (((64 * a - 4 + rl) * D + 64 * b) * 2 + 16 * (sl & 1) + 32 * ((sl >> 1) ^ ((rl >> 1) & 1))), LB + A_K);
      dma1((const GAS char*)p.k + uk, LB + A_K + 512 + w * 1024);
      dma1((const GAS char*)p.v + uv, LB + A_V + w * 1024);
      ALOAD("global_load_dwordx2", shw, (const GAS char*)p.shk + (unsigned)ch * 8);
      dma1((const GAS char*)p.q + uv, LB + A_Q + w * 1024);
      asm volatile("global_load_dword %0, %1, off sc1" : "=v"(tag) : "v"((const GAS char*)p.ws + OFF_EP) : "memory");
      ALOAD("global_load_dwordx4", qs, (const GAS char*)p.q + (unsigned)(srow * D + 8 * lane) * 2);
      {
        const int hidx = (lane & 31) + 2 * q4 - 30;
        ALOAD("global_load_ushort", hval, (const GAS u16*)(p.ssk + (size_t)bch * S + (hidx < 0 ? 0 : hidx)));
        ALOAD("global_load_ushort", dv, (const GAS u16*)(p.ssd + bch));
      }
      {
        const unsigned i = (unsigned)bid * 5 + (unsigned)(lane < 5 ? lane : 0);
        const unsigned off = i < 608 ? 4096 + i * 128 : OFF_ST + (i - 608 < 512 ? i - 608 : 0) * 128;
        if (w == 7) asm volatile("global_load_dword %0, %1, off sc1" : "=v"(dm1) : "v"((const GAS char*)p.ws + off) : "memory");
      }
    }
    if (a == 0 && tid < 32) *(LAS v4u*)(L + A_K + 16 * tid) = v4u{0, 0, 0, 0};
    if (tid < 16) *(LAS unsigned*)(L + L_FLAG + 4 * tid) = 0u;
    if (tid < 128) *(LAS v4u*)(L + B_XC + (tid >> 5) * B_XCS + 4096 + 16 * (tid & 31)) = v4u{0, 0, 0, 0};
    if (TOUCH && w == 7) { WAIT_V(5); } else { WAIT_V(4); }
    asm volatile("" : "+v"(shw) :: "memory");
    BARRIER();
    STAMPT(1);
    const int s0 = 64 * a + 8 * seg;
    const int r0 = 8 * seg + tq;
    const unsigned fr0 = (r0 >> 1) & 1, sg = seg & 1;
    const unsigned ua = ((w & 3) ^ (fr0 | (sg << 1))) * 32 + 8 * tp, ub = ((w & 3) ^ (fr0 | ((sg ^ 1) << 1))) * 32 + 8 * tp;
    {
      LAS unsigned char* kb = L + A_K + r0 * 128;
      const v2u k0 = trread(kb + ua), k1 = trread(kb + 512 + ua), k2 = trread(kb + 1024 + ub);
      LAS unsigned char* vb = L + A_V + r0 * 128 + ua;
      const v2u v0 = trread(vb), v1 = trread(vb + 512);
      LAS unsigned char* qb = L + A_Q + r0 * 128 + ua;
      const v2u q0 = trread(qb), q1 = trread(qb + 512);
      const unsigned E[6] = {k0.x, k0.y, k1.x, k1.y, k2.x, k2.y};
      unsigned O[5];
#pragma unroll
      for (int i = 0; i < 5; ++i) O[i] = __builtin_amdgcn_alignbit(E[i + 1], E[i], 16);
      const unsigned shx = shw.x, shy = shw.y;
      const unsigned w32 = __builtin_amdgcn_alignbit(shy, shy, 16);
      const unsigned w10 = __builtin_amdgcn_alignbit(shx, shx, 16);
      float ks[8];
#pragma unroll
      for (int j = 0; j < 8; ++j) {
        const unsigned pa = (j & 1) ? E[(j + 1) >> 1] : O[j >> 1];
        const unsigned pb = (j & 1) ? E[(j + 3) >> 1] : O[(j + 2) >> 1];
        ks[j] = dot2(pb, w10, dot2(pa, w32, 0.f));
      }
      const f32x2 a0 = f32x2{ks[0], ks[1]} * up2(v0.x), a1 = f32x2{ks[2], ks[3]} * up2(v0.y);
      const f32x2 a2 = f32x2{ks[4], ks[5]} * up2(v1.x), a3 = f32x2{ks[6], ks[7]} * up2(v1.y);
      const v4u xr = {pk(a3.y, a3.x), pk(a2.y, a2.x), pk(a1.y, a1.x), pk(a0.y, a0.x)};
      st16(xr, rs, OFF_XR + (ch * S + (S - 8 - s0)) * 2);
      st16(v4u{q0.x, q0.y, q1.x, q1.y}, rs, OFF_XT + (ch * S + s0) * 2);
    }
    asm volatile("s_waitcnt vmcnt(2)" : "+v"(qs), "+v"(tag), "+v"(hval), "+v"(dv), "+v"(dm1) :: "memory");
    tag += 1u;
    {
      float s1 = dot2(qs.x, ONES, dot2(qs.y, ONES, 0.f)) + dot2(qs.z, ONES, dot2(qs.w, ONES, 0.f));
      float s2 = dot2(qs.x, qs.x, dot2(qs.y, qs.y, 0.f)) + dot2(qs.z, qs.z, dot2(qs.w, qs.w, 0.f));
      s1 = wavesum(s1); s2 = wavesum(s2);
      const float mean = s1 * (1.0f / D);
      float var = __builtin_fmaf(-mean, mean, s2 * (1.0f / D));
      var = var > 0.f ? var : 0.f;
      const float rstd = __builtin_amdgcn_rsqf(var + 1e-5f);
      if (lane == 63) __builtin_amdgcn_raw_buffer_store_b64(v2u{pk(-mean * rstd, rstd), tag}, rs, OFF_ST + 8 * srow, 0, SC1);
    }
    STAMPT(2);
    WAIT_V(1);
    BARRIER();
    if (tid == 0) st4(tag, rs, OFF_FA1 + (b * 32 + a) * 16);
    STAMPT(3);
    if (w >= 4) {
      const int w4 = w - 4, wb = bid < 224 ? bid : 0;
#pragma unroll
      for (int cc = 0; cc < 2; ++cc) {
        const int cw = 2 * w4 + cc;
        const u16* hrow = p.ssk + (size_t)(2 * cj + (cw >> 2)) * S;
        const int idx = 8 * (4 + lane) + 2 * (cw & 3) - 30;
        const unsigned lb = LB + B_HC + cw * B_HCS + 64;
        dma3((const GAS void*)(hrow + idx), lb);
        int idx2 = idx + 3 * 512;
        idx2 = idx2 > S - 8 ? S - 8 : idx2;
        dma1((const GAS void*)(hrow + idx2), lb + 3072);
      }
#pragma unroll
      for (int cc = 0; cc < 2; ++cc) {
        const int wr = 16 * cc + (lane >> 2);
        dma1((const GAS char*)p.w1 + (unsigned)((32 * (wb / 14) + wr) * F + 128 * (wb % 14) + 32 * w4 + 8 * (lane & 3)) * 2, LB + A_W1 + w4 * 2048 + cc * 1024);
        dma1((const GAS char*)p.w2 + (unsigned)((32 * (wb >> 2) + wr) * D + 128 * (wb & 3) + 32 * w4 + 8 * (lane & 3)) * 2, LB + A_W2 + w4 * 2048 + cc * 1024);
      }
    }
    if (w == 0) poll1(rs, OFF_FA1 + (xcd * 32 + (lane & 31)) * 16, tag);
    STAMPT(27);
    BARRIER();
    STAMPT(4);
  }

  v4u wf2[28];
  unsigned b1n;
  unsigned pf0 = 0, pf1 = 0, pf2 = 0, pf3 = 0;
  const int kh = w >> 2, nq = w & 3;
  const unsigned w1vo = OFF_W1 + (d_nt * 8 + w) * 16 * 1024 + lane * 16;
  auto w1load = [&](int i) {
    const unsigned vo = w1vo + (((i >> 2) + nq) & 3) * 4096;
    if ((i & 3) == 0) GLD(wf1[i], vo, p.ws, 0);
    if ((i & 3) == 1) GLD(wf1[i], vo, p.ws, 1024);
    if ((i & 3) == 2) GLD(wf1[i], vo, p.ws, 2048);
    if ((i & 3) == 3) GLD(wf1[i], vo, p.ws, 3072);
  };
  {
    const int c = i16, al = c & 1;
    const int sa = 256 * q4 + 8 * g + 32 * (c >> 1) + 4 * al;
    v2u xt0, xt1;
    {
      const int cp = w >> 1;
      const GAS char* xs = (const GAS char*)p.ws + OFF_XR + (2 * cj + (cp >> 1)) * (S * 2) + 2 * (1 - (cp & 1)) + (w & 1) * 2048 + lane * 16;
      dma2s(xs, LB + B_XC + cp * B_XCS + (w & 1) * 2048);
    }
    asm volatile("global_load_dwordx2 %0, %1, off sc1" : "=v"(xt0) : "v"((const GAS char*)p.ws + OFF_XT + (bch * S + sa) * 2) : "memory");
    asm volatile("global_load_dwordx2 %0, %1, off offset:2048 sc1" : "=v"(xt1) : "v"((const GAS char*)p.ws + OFF_XT + (bch * S + sa) * 2) : "memory");
    dma2s((const GAS char*)p.ws + OFF_ST + w * 2048 + lane * 16, LB + B_ST + w * 2048);
    const float dch = bflo(dv);
    if (lane < 32) *(LAS u16*)(L + B_HC + w * B_HCS + 2 * lane) = ((lane & 31) + 2 * q4 - 30) >= 0 ? (u16)hval : (u16)0;
    WAIT_V(4);
    if ((w & 3) == 1 && lane == 0) *(LAS u16*)(L + B_XC + (w >> 1) * B_XCS + 2 * (S - 1)) = 0;
    BARRIER();
    STAMPT(5);

    LAS unsigned char* ab = L + B_HC + (chl * 4 + (c & 3)) * B_HCS + 2 * (224 + 8 * (c >> 2) + 8 * g) - 128 * q4;
    LAS unsigned char* bb = L + B_XC + (chl * 2 + al) * B_XCS + 2 * (2240 - 1792 - 32 * (c >> 1) + 8 * g) - 128 * q4;
    LAS f32x4* red = (LAS f32x4*)(L + B_RED);
    const int rw = w;
    auto bfinish = [&](int hf, v2u xt) {
      LAS f32x4* rp = red + (chl * 32 + hf * 4 + q4) * 64 + lane;
      const f32x4 a0 = rp[0], a1 = rp[8 * 64], a2 = rp[16 * 64], a3 = rp[24 * 64];
      const int s = sa + 1024 * hf;
      const v2u kvr = *(LAS v2u*)(L + B_XC + (chl * 2 + 1) * B_XCS + 2 * (S - 4 - s));
      v4u g0 = *(LAS v4u*)(L + B_ST + 8 * s), g1 = *(LAS v4u*)(L + B_ST + 8 * s + 16);
      if (__builtin_amdgcn_ballot_w64(g0.y == tag && g0.w == tag && g1.y == tag && g1.w == tag) != ~0ull) {
        for (int spins = 0; spins < SPIN_CAP; ++spins) {
          unsigned long long e[4];
#pragma unroll
          for (int i = 0; i < 4; ++i) e[i] = __hip_atomic_load((const GAS unsigned long long*)(p.ws + OFF_ST + 8 * s) + i, __ATOMIC_RELAXED, __HIP_MEMORY_SCOPE_AGENT);
          g0 = v4u{(unsigned)e[0], (unsigned)(e[0] >> 32), (unsigned)e[1], (unsigned)(e[1] >> 32)};
          g1 = v4u{(unsigned)e[2], (unsigned)(e[2] >> 32), (unsigned)e[3], (unsigned)(e[3] >> 32)};
          if (__builtin_amdgcn_ballot_w64(g0.y == tag && g0.w == tag && g1.y == tag && g1.w == tag) == ~0ull) break;
          __builtin_amdgcn_s_sleep(2);
        }
      }
      const unsigned u0 = g0.x, u1 = g0.z, u2 = g1.x, u3 = g1.z;
      const f32x2 mr01 = {bflo(u0), bflo(u1)}, rr01 = {bfhi(u0), bfhi(u1)}, mr23 = {bflo(u2), bflo(u3)}, rr23 = {bfhi(u2), bfhi(u3)};
      const f32x2 ylo = (a0.xy + a1.xy) + (a2.xy + a3.xy), yhi = (a0.zw + a1.zw) + (a2.zw + a3.zw);
      const float s0_ = al ? ylo.x : yhi.x, s1_ = al ? ylo.y : yhi.y;
      const float r0_ = asf(__builtin_amdgcn_update_dpp(0, asu(s0_), 0xB1, 0xf, 0xf, true));
      const float r1_ = asf(__builtin_amdgcn_update_dpp(0, asu(s1_), 0xB1, 0xf, 0xf, true));
      const f32x2 y01 = {al ? r0_ : ylo.x, al ? yhi.x : r0_}, y23 = {al ? r1_ : ylo.y, al ? yhi.y : r1_};
      const unsigned k1 = kvr.y, k0 = kvr.x;
      const f32x2 x01 = up2(xt.x), x23 = up2(xt.y);
      const f32x2 dd = {dch, dch};
      const f32x2 t01 = fma2(f32x2{bfhi(k1), bflo(k1)}, dd, y01), t23 = fma2(f32x2{bfhi(k0), bflo(k0)}, dd, y23);
      const f32x2 h01 = fma2(fma2(x01, rr01, mr01), t01, x01), h23 = fma2(fma2(x23, rr23, mr23), t23, x23);
      const int sl = (s & 63) >> 2;
      __builtin_amdgcn_raw_buffer_store_b64(v2u{pk(h01.x, h01.y), pk(h23.x, h23.y)}, rs,
                                            OFF_HT + ((s >> 6) * D + bch) * 128 + ((sl ^ (((bch >> 1) & 1) << 3)) << 3), 0, SC1);
    };
    {
      v4u af[8], bf[8], ag[8], bg[8];
      f32x4 acc[4];
#pragma unroll
      for (int i = 0; i < 4; ++i) acc[i] = f32x4{0.f, 0.f, 0.f, 0.f};
#pragma unroll
      for (int i = 0; i < 4; ++i) { bf[i] = *(LAS v4u*)(bb + 512 * (7 - i)); af[i] = *(LAS v4u*)(ab + 512 * i); }
#pragma unroll
      for (int i = 0; i < 4; ++i) { bg[i] = *(LAS v4u*)(bb - 64 + 512 * (7 - i)); ag[i] = *(LAS v4u*)(ab - 64 + 512 * i); }
      SCHED();
#pragma unroll
      for (int ip = 0; ip < 4; ++ip)
#pragma unroll
        for (int i = ip; i < 4; ++i) acc[i] = mfma16(af[i - ip], bf[ip], acc[i]);
#pragma unroll
      for (int i = 4; i < 8; ++i) { bf[i] = *(LAS v4u*)(bb + 512 * (7 - i)); af[i] = *(LAS v4u*)(ab + 512 * i); }
      SCHED();

#pragma unroll
      for (int i = 0; i < 4; ++i)
#pragma unroll
        for (int ip = 0; ip <= i; ++ip) acc[i] = mfma16(ag[i - ip], bg[ip], acc[i]);
      SCHED();
      asm volatile("s_waitcnt vmcnt(0)" : "+v"(xt0), "+v"(xt1) :: "memory");
      if (w >= 4 && bid < 224) {
#pragma unroll
        for (int cc = 0; cc < 2; ++cc) {
          const int v = w & 3;
          const int off = v * 2048 + (16 * cc + 8 * hh + tq) * 64 + 32 * blk + 16 * (tp & 1) + 8 * (tp >> 1);
          const v4u f1 = trfrag(L + A_W1 + off, 256);
          const v4u f2 = trfrag(L + A_W2 + off, 256);
          const int k16 = 2 * (bid / 14) + cc, nt = bid % 14;
          st16(f1, rs, OFF_W1 + (((nt * 8 + (k16 >> 4) * 4 + v) * 16) + (k16 & 15)) * 1024 + lane * 16);
          const int ka = 2 * (bid >> 2) + cc, dt = 2 * (bid & 3) + (v >> 1);
          st16(f2, rs, OFF_W2 + (((dt * 8 + ka / 14) * 28) + (ka % 14) * 2 + (v & 1)) * 1024 + lane * 16);
        }
      }
#pragma unroll
      for (int i = 0; i < 4; ++i) red[(rw * 8 + i) * 64 + lane] = acc[i];
      BARRIER();
      STAMPT(21);
      f32x4 ac2[4];
#pragma unroll
      for (int i = 0; i < 4; ++i) ac2[i] = f32x4{0.f, 0.f, 0.f, 0.f};
      bfinish(0, xt0);
      STAMPT(26);
      SCHED();
#pragma unroll
      for (int ip = 0; ip < 8; ++ip) {
#pragma unroll
        for (int i = (ip > 4 ? ip : 4); i < 8; ++i) ac2[i - 4] = mfma16(af[i - ip], bf[ip], ac2[i - 4]);
        SCHED();
        if (ip < 4) { ag[4 + ip] = *(LAS v4u*)(ab - 64 + 512 * (4 + ip)); bg[4 + ip] = *(LAS v4u*)(bb - 64 + 512 * (7 - 4 - ip)); }
        SCHED();
      }
      WAIT_V(0);
      lds_count_flag8(L, 4, lane, tag, rs, OFF_FB0 + bid * 16);
      asm volatile("global_load_ushort %0, %1, %2" : "=v"(b1n) : "v"((unsigned)(128 * d_nt + 32 * nq + (lane & 31)) * 2), "s"(p.b1) : "memory");
      SCHED();
#pragma unroll
      for (int i = 4; i < 8; ++i) {
#pragma unroll
        for (int ip = 0; ip <= i; ++ip) {
          ac2[i - 4] = mfma16(ag[i - ip], bg[ip], ac2[i - 4]);
        }
        SCHED();
        if (i > 4) red[(rw * 8 + 4 + i - 5) * 64 + lane] = ac2[i - 5];
        SCHED();
      }
      red[(rw * 8 + 7) * 64 + lane] = ac2[3];
    }
    BARRIER();
    STAMPT(6);
    if (w == 0) LOOK_ISSUE(OFF_FB0 + xcd * 4096 + lane * 16);
    bfinish(1, xt1);
    STAMPT(7);
    if (w == 0) LOOK_CHECK(1, OFF_FB0 + xcd * 4096 + lane * 16);
    STAMPT(8);
  }

  {
    BARRIER();
    STAMPT(9);
    const GAS unsigned char* tsrc = (const GAS unsigned char*)p.ws + OFF_HT + (size_t)d_mt * 65536 + w * 8192 + lane * 16;
    const unsigned w2off = OFF_W2 + (e_dt * 8 + w) * 28 * 1024 + lane * 16;
    auto second_wait = [&]() {
      if (w == 0) { poll4(rs, OFF_FB1 + xcd * 4096 + lane * 16, tag); lds_set(L, 0); }
      lds_wait(L, 0);
    };
    auto second_res = [&]() {
      const int rr = 16 * (w & 3) + (lane >> 2);
      const int dch2 = 64 * e_dt + rr;
      dma1s((const GAS unsigned char*)p.ws + OFF_HT + ((16 * (w >> 2) + e_mt) * D + dch2) * 128 + (((unsigned)e_j ^ ((dch2 >> 1) & 1)) << 6) + 16 * (lane & 3),
            LB + E_RES + w * 1024);
    };
    if (d_act) { dma4s(tsrc, LB + D_TILE + w * 8192); dma4s(tsrc + 4096, LB + D_TILE + w * 8192 + 4096); }
    if (d_act) { WAIT_V(8); } else { WAIT_V(0); }
    lds_count_flag8(L, 5, lane, tag, rs, OFF_FB1 + bid * 16);
    if (d_act) {
      asm volatile("" : "+v"(b1n) :: "memory");
#pragma unroll
      for (int i = 0; i < 8; ++i) w1load(i);
      SCHED();
      const unsigned basel = (unsigned)(D_TILE + (256 * kh + 8 * hh + tq) * 128 + 32 * blk + 8 * tp);
      const unsigned fsw = ((tq >> 1) & 1) << 6;
      const unsigned adr[2] = {basel + ((unsigned)(kh << 6) ^ fsw), basel + ((unsigned)((kh ^ 1) << 6) ^ fsw)};
      f32x16 acc[2][2];
      auto fraddr = [&](int c, int m) { return L + (adr[m & 1] + (unsigned)(c * 65536 + 8192 * ((((m >> 1) >> 2) + nq) & 3)) + 2048 * ((m >> 1) & 3)); };
      float xg[16];
      unsigned og[8];
      auto hstore = [&](int c) {
        const unsigned ho = OFF_HF + ((32 * c + 2 * d_mt + kh) * 112 + 8 * d_nt + 2 * nq) * 1024 + lane * 16;
        st16(v4u{og[0], og[1], og[2], og[3]}, rs, ho);
        st16(v4u{og[4], og[5], og[6], og[7]}, rs, ho + 1024);
      };
      v2u sg[2][2];
      auto sload = [&](int c) {
        const GAS char* sp = (const GAS char*)p.ws + OFF_HS + (1024 * c + 64 * d_mt + 32 * kh + (lane & 31)) * 16;
        asm volatile("global_load_dwordx2 %0, %1, off sc1" : "=v"(sg[c][0]) : "v"(sp) : "memory");
        asm volatile("global_load_dwordx2 %0, %1, off offset:8 sc1" : "=v"(sg[c][1]) : "v"(sp) : "memory");
      };
      auto mstep = [&](int c) {
#pragma unroll
        for (int r = 0; r < 2; ++r)
#pragma unroll
          for (int e = 0; e < 16; ++e) acc[c][r][e] = 0.f;
        v4u fr[4];
#pragma unroll
        for (int n = 0; n < 3; ++n) fr[n] = trfrag(fraddr(c, n), 512);
        SCHED();
#pragma unroll
        for (int n = 0; n < 32; ++n) {
          const int i = n >> 1, r = n & 1;
          if (c == 0 && r == 0) {
            if (n < 16) { w1load(8 + i); WAITF(8, wf1[i]); }
            if (n == 16) {
              WAITF(7, wf1[8]);
              if (w == 0) { LOOK_CHECK(4, OFF_FB1 + xcd * 4096 + lane * 16); lds_set(L, 0); }
              lds_wait(L, 0);
            }
            if (n == 18) WAITF(7, wf1[9]);
            if (n == 20) WAITF(7, wf1[10]);
            if (n == 22) WAITF(7, wf1[11]);
            if (n == 24) WAITF(7, wf1[12]);
            if (n == 26) WAITF(7, wf1[13]);
            if (n == 28) WAITF(7, wf1[14]);
            if (n == 30) WAITF(9, wf1[15]);
          }
          if (c == 0 && n == 7 && w == 0) LOOK_ISSUE(OFF_FB1 + xcd * 4096 + lane * 16);
          if (c == 0 && n >= 16 && (n & 1) == 0) dma1s(tsrc + 16 * 65536 + ((n - 16) >> 1) * 1024, LB + D_TILE + 65536 + w * 8192 + ((n - 16) >> 1) * 1024);
          if (c == 1 && n == 1) second_res();
          if (c == 1 && n == 15) sload(1);
          if (c == 0 && n == 29) {
            sload(0);
            if (w == 0) dma1s((const GAS char*)p.ws + OFF_CS + d_nt * 1024 + lane * 16, LB + D_CSG);
          }
          if (n + 3 < 32) fr[(n + 3) & 3] = trfrag(fraddr(c, n + 3), 512);
          acc[c][r] = mfma32(wf1[i], fr[n & 3], acc[c][r]);
          if (c == 1 && (n & 1) == 0) wf2[n >> 1] = ld16(rs, w2off + (n >> 1) * 1024);
          SCHED();
        }
      };
      auto xwrite = [&](int c) {
        LAS f32x4* ex = (LAS f32x4*)(L + D_TILE + c * 32768) + w * 256 + lane;
#pragma unroll
        for (int qd = 0; qd < 4; ++qd) ex[qd * 64] = f32x4{acc[c][1][4 * qd], acc[c][1][4 * qd + 1], acc[c][1][4 * qd + 2], acc[c][1][4 * qd + 3]};
      };
      auto xpre = [&](int c) {
        LAS f32x4* ex = (LAS f32x4*)(L + D_TILE + c * 32768) + (w ^ 4) * 256 + lane;
        LAS f32x4* cb = (LAS f32x4*)(L + D_CB) + 16 * nq + 4 * hh;
        f32x4 pp[4], c0[4], c1[4];
#pragma unroll
        for (int qd = 0; qd < 4; ++qd) { pp[qd] = ex[qd * 64]; c0[qd] = cb[8 * (qd >> 1) + 2 * (qd & 1)]; c1[qd] = cb[8 * (qd >> 1) + 2 * (qd & 1) + 1]; }
        SCHED();
        unsigned u0 = sg[c][0].x, u1 = sg[c][1].x;
        if (__builtin_amdgcn_ballot_w64(((sg[c][0].y ^ tag) | (sg[c][1].y ^ tag)) != 0u) != 0ull) {
          const GAS unsigned long long* sp = (const GAS unsigned long long*)(p.ws + OFF_HS + (1024 * c + 64 * d_mt + 32 * kh + (lane & 31)) * 16);
          for (int spins = 0; spins < SPIN_CAP; ++spins) {
            const unsigned long long e0 = __hip_atomic_load(sp, __ATOMIC_RELAXED, __HIP_MEMORY_SCOPE_AGENT), e1 = __hip_atomic_load(sp + 1, __ATOMIC_RELAXED, __HIP_MEMORY_SCOPE_AGENT);
            u0 = (unsigned)e0; u1 = (unsigned)e1;
            if (__builtin_amdgcn_ballot_w64((((unsigned)(e0 >> 32) ^ tag) | ((unsigned)(e1 >> 32) ^ tag)) != 0u) == 0ull) break;
            __builtin_amdgcn_s_sleep(2);
          }
        }
        const float rstd = asf(u0), nm = asf(u1);
        const f32x2 rs2 = {rstd, rstd}, nm2 = {nm, nm};
#pragma unroll
        for (int qd = 0; qd < 4; ++qd)
#pragma unroll
          for (int hf = 0; hf < 2; ++hf) {
            const int e = 2 * qd + hf;
            const f32x4 cc = hf ? c1[qd] : c0[qd];
            const f32x2 a = f32x2{acc[c][0][2 * e], acc[c][0][2 * e + 1]} + (hf ? pp[qd].zw : pp[qd].xy);
            const f32x2 x = fma2(rs2, a, fma2(nm2, cc.xz, cc.yw));
            xg[2 * e] = x.x; xg[2 * e + 1] = x.y;
          }
      };
      auto cbtable = [&]() {
        const int n = 32 * nq + (lane & 31);
        v2u gq = *(LAS v2u*)(L + D_CSG + 8 * n);
        unsigned cs = gq.x;
        if (__builtin_amdgcn_ballot_w64(gq.y != tag) != 0ull) {
          for (int spins = 0; spins < SPIN_CAP; ++spins) {
            const unsigned long long e0 = __hip_atomic_load((const GAS unsigned long long*)(p.ws + OFF_CS) + 128 * d_nt + n, __ATOMIC_RELAXED, __HIP_MEMORY_SCOPE_AGENT);
            cs = (unsigned)e0;
            if (__builtin_amdgcn_ballot_w64((unsigned)(e0 >> 32) != tag) == 0ull) break;
            __builtin_amdgcn_s_sleep(2);
          }
        }
        *(LAS f32x2*)(L + D_CB + 8 * n) = f32x2{asf(cs), bflo(b1n)};
        asm volatile("s_waitcnt lgkmcnt(0)" ::: "memory");
      };
      auto gelu_all = [&]() {
#pragma unroll
        for (int e = 0; e < 8; ++e) {
          const f32x2 x = {xg[2 * e], xg[2 * e + 1]};
          const f32x2 pz = fma2(x * x, f32x2{-0.10294324f, -0.10294324f}, f32x2{-2.3022082f, -2.3022082f});
          const f32x2 z = x * pz;
          const f32x2 den = f32x2{__builtin_amdgcn_exp2f(z.x), __builtin_amdgcn_exp2f(z.y)} + f32x2{1.f, 1.f};
          const f32x2 y = x * f32x2{__builtin_amdgcn_rcpf(den.x), __builtin_amdgcn_rcpf(den.y)};
          og[e] = pk(y.x, y.y);
        }
      };
      WAIT_V(8);
      BARRIER();
      STAMPT(10);
      SCHED();
      mstep(0);
      STAMPT(22);
      BARRIER();
      STAMPT(11);
      xwrite(0);
      asm volatile("s_waitcnt vmcnt(0)" : "+v"(sg[0][0]), "+v"(sg[0][1]) :: "memory");
      BARRIER();
      STAMPT(23);
      cbtable();
      xpre(0);
      gelu_all();
      hstore(0);
      BARRIER();
      STAMPT(24);
      mstep(1);
      STAMPT(25);
      xwrite(1);
      asm volatile("s_waitcnt vmcnt(8)" : "+v"(sg[1][0]), "+v"(sg[1][1]) :: "memory");
      lds_count_flag(L, 2, 8, tid, d_act, tag, rs, OFF_FD + d_mt * 256 + d_nt * 16);
      BARRIER();
      STAMPT(12);
      xpre(1);
      gelu_all();
      hstore(1);
      STAMPT(13);
    }
    asm volatile("" ::: "memory");
    if (!d_act) {
      const int u = xcd * 4 + (bix - 28);
      if (u >= 16) {
        const int kg = tid >> 4, cc = tid & 15;
        f32x2 cs[4] = {{0.f, 0.f}, {0.f, 0.f}, {0.f, 0.f}, {0.f, 0.f}};
        if (cc < 14) {
          v4u wv[16];
#pragma unroll
          for (int i = 0; i < 16; ++i) wv[i] = *(const GAS v4u*)((const GAS char*)p.w1 + (unsigned)((16 * kg + i) * F + 112 * (u - 16) + 8 * cc) * 2);
#pragma unroll
          for (int i = 0; i < 16; ++i) { cs[0] += up2(wv[i].x); cs[1] += up2(wv[i].y); cs[2] += up2(wv[i].z); cs[3] += up2(wv[i].w); }
        }
        LAS f32x4* t = (LAS f32x4*)(L + 65536) + (kg * 16 + cc) * 2;
        t[0] = f32x4{cs[0].x, cs[0].y, cs[1].x, cs[1].y}; t[1] = f32x4{cs[2].x, cs[2].y, cs[3].x, cs[3].y};
        BARRIER();
        if (tid < 112) {
          float a = 0.f;
#pragma unroll
          for (int i = 0; i < 32; ++i) a += *(LAS float*)(L + 65536 + (i * 128 + tid) * 4);
          __builtin_amdgcn_raw_buffer_store_b64(v2u{asu(a), tag}, rs, OFF_CS + 8 * (112 * (u - 16) + tid), 0, SC1);
        }
      }
      if (u >= 16) second_wait();
      {
        const GAS unsigned char* src = (const GAS unsigned char*)p.ws + OFF_HT + (size_t)u * 65536 + w * 8192 + lane * 16;
        dma4s(src, LB + w * 8192); dma4s(src + 4096, LB + w * 8192 + 4096);
      }
      WAIT_V(0);
      {
        float s1[2][2] = {{0.f, 0.f}, {0.f, 0.f}}, s2[2][2] = {{0.f, 0.f}, {0.f, 0.f}};
        LAS unsigned char* sp = L + w * 8192 + (8 * hh + tq) * 128 + 32 * blk + 8 * tp;
        const unsigned fsw = ((tq >> 1) & 1) << 6;
#pragma unroll
        for (int n = 0; n < 8; ++n) {
          const int r = n & 1;
          const v4u hf = trfrag(sp + (n >> 1) * 2048 + (((unsigned)r << 6) ^ fsw), 512);
          s1[r][0] = dot2(hf.x, ONES, s1[r][0]); s2[r][0] = dot2(hf.x, hf.x, s2[r][0]);
          s1[r][1] = dot2(hf.y, ONES, s1[r][1]); s2[r][1] = dot2(hf.y, hf.y, s2[r][1]);
          s1[r][0] = dot2(hf.z, ONES, s1[r][0]); s2[r][0] = dot2(hf.z, hf.z, s2[r][0]);
          s1[r][1] = dot2(hf.w, ONES, s1[r][1]); s2[r][1] = dot2(hf.w, hf.w, s2[r][1]);
        }
#pragma unroll
        for (int r = 0; r < 2; ++r) {
          const float a = halfsum(s1[r][0] + s1[r][1]), b = halfsum(s2[r][0] + s2[r][1]);
          if (lane < 32) *(LAS f32x2*)(L + 98304 + (w * 64 + 32 * r + lane) * 8) = f32x2{a, b};
        }
      }
      BARRIER();
      if (w == 0) {
        f32x2 sa = {0.f, 0.f};
#pragma unroll
        for (int ww = 0; ww < 8; ++ww) sa += *(LAS f32x2*)(L + 98304 + (ww * 64 + lane) * 8);
        const float mean = sa.x * (1.0f / D);
        float var = __builtin_fmaf(-mean, mean, sa.y * (1.0f / D));
        var = var > 0.f ? var : 0.f;
        const float r1 = __builtin_amdgcn_rsqf(var + 1e-5f);
        const float rstd = r1 * __builtin_amdgcn_rsqf(__builtin_fmaf(var * r1, r1, 1e-5f));
        __builtin_amdgcn_raw_buffer_store_b64(v2u{asu(rstd), tag}, rs, OFF_HS + (64 * u + lane) * 16, 0, SC1);
        __builtin_amdgcn_raw_buffer_store_b64(v2u{asu(-rstd * mean), tag}, rs, OFF_HS + (64 * u + lane) * 16 + 8, 0, SC1);
      }
      if (u < 16) second_wait();
      second_res();
#pragma unroll
      for (int i = 0; i < 16; ++i) wf2[i] = ld16(rs, w2off + i * 1024);
    }
  }

  {
    const int sbo = w >> 2, dbo = (w >> 1) & 1, hf = w & 1;
    if (w == 0) poll1(rs, OFF_FD + e_mt * 256 + (lane < 14 ? lane : 0) * 16, tag);
    const unsigned w2off = OFF_W2 + (e_dt * 8 + w) * 28 * 1024 + lane * 16;
#pragma unroll
    for (int i = 16; i < 28; ++i) wf2[i] = ld16(rs, w2off + i * 1024);
    const v4u b2v = *(const GAS v4u*)((const GAS char*)p.b2 + (unsigned)(64 * e_dt + 32 * dbo + 16 * hf + 8 * hh) * 2);
    SCHED();
    WAIT_V(13);
    BARRIER();
    if (d_act && tid == 0) st4(tag, rs, OFF_FD + 4096 + d_mt * 256 + d_nt * 16);
    STAMPT(14);
    const unsigned hoff0 = OFF_HF + ((2 * e_mt + e_j) * 112 + 14 * w) * 1024 + lane * 16, hoff1 = hoff0 + 32 * 112 * 1024;
    if (w == 0) { poll1(rs, OFF_FD + 4096 + e_mt * 256 + (lane < 14 ? lane : 0) * 16, tag); lds_set(L, 1); }
    v4u hf0[14], hf1[14];
#pragma unroll
    for (int k = 0; k < 14; ++k) hf0[k] = ld16(rs, hoff0 + k * 1024);
    SCHED();
    lds_wait(L, 1);
    STAMPT(15);
    LAS f32x4* red = (LAS f32x4*)(L + E_RED + w * 16384) + lane;
    {
      f32x16 acc[2];
#pragma unroll
      for (int e = 0; e < 16; ++e) { acc[0][e] = 0.f; acc[1][e] = 0.f; }
#pragma unroll
      for (int k = 0; k < 14; ++k) {
        acc[0] = mfma32(wf2[2 * k], hf0[k], acc[0]); acc[1] = mfma32(wf2[2 * k + 1], hf0[k], acc[1]);
        SCHED();
        hf1[k] = ld16(rs, hoff1 + k * 1024);
        SCHED();
      }
#pragma unroll
      for (int d = 0; d < 2; ++d)
#pragma unroll
        for (int qd = 0; qd < 4; ++qd) red[(d * 4 + qd) * 64] = f32x4{acc[d][4 * qd], acc[d][4 * qd + 1], acc[d][4 * qd + 2], acc[d][4 * qd + 3]};
    }
    STAMPT(16);
    {
      f32x16 acc[2];
#pragma unroll
      for (int e = 0; e < 16; ++e) { acc[0][e] = 0.f; acc[1][e] = 0.f; }
#pragma unroll
      for (int k = 0; k < 14; ++k) { acc[0] = mfma32(wf2[2 * k], hf1[k], acc[0]); acc[1] = mfma32(wf2[2 * k + 1], hf1[k], acc[1]); }
#pragma unroll
      for (int d = 0; d < 2; ++d)
#pragma unroll
        for (int qd = 0; qd < 4; ++qd) red[512 + (d * 4 + qd) * 64] = f32x4{acc[d][4 * qd], acc[d][4 * qd + 1], acc[d][4 * qd + 2], acc[d][4 * qd + 3]};
    }
    BARRIER();
    STAMPT(17);
    f32x2 o[4] = {{0.f, 0.f}, {0.f, 0.f}, {0.f, 0.f}, {0.f, 0.f}};
#pragma unroll
    for (int ww = 0; ww < 8; ++ww) {
      LAS f32x4* rp = (LAS f32x4*)(L + E_RED + ww * 16384) + sbo * 512 + (dbo * 4 + 2 * hf) * 64 + lane;
      const f32x4 a = rp[0], b = rp[64];
      o[0] += a.xy; o[1] += a.zw; o[2] += b.xy; o[3] += b.zw;
    }
    const v4u hr = trfrag(L + E_RES + sbo * 4096 + (32 * dbo + 16 * hf + 8 * hh + tq) * 64 + 32 * blk + 8 * tp, 256);
    o[0] += up2(b2v.x) + up2(hr.x); o[1] += up2(b2v.y) + up2(hr.y);
    o[2] += up2(b2v.z) + up2(hr.z); o[3] += up2(b2v.w) + up2(hr.w);
    const v4u ov = {pk(o[0].x, o[0].y), pk(o[1].x, o[1].y), pk(o[2].x, o[2].y), pk(o[3].x, o[3].y)};
    {
      const rsrc_t ro = __builtin_amdgcn_make_buffer_rsrc((void*)p.out, 0, S * D * 2, 0x00020000);
      __builtin_amdgcn_raw_buffer_store_b128(ov, ro, (unsigned)((1024 * sbo + 64 * e_mt + 32 * e_j + (lane & 31)) * D + 64 * e_dt + 32 * dbo + 16 * hf + 8 * hh) * 2, 0, OUTSC1 ? SC1 : 0);
    }
    if (bid == 0 && tid == 0) st4(tag, rs, OFF_EP);
    STAMPT(18);
  }
}

extern "C" void kernel_launch(void* const* d_in, const int* in_sizes, int n_in, void* d_out, int out_size, void* d_ws,
                              size_t ws_size, hipStream_t stream) {
  Params p;
  p.q = (const u16*)d_in[0]; p.k = (const u16*)d_in[1]; p.v = (const u16*)d_in[2];
  p.shk = (const u16*)d_in[3]; p.ssk = (const u16*)d_in[4]; p.ssd = (const u16*)d_in[5];
  p.w1 = (const u16*)d_in[7]; p.b1 = (const u16*)d_in[8]; p.w2 = (const u16*)d_in[9]; p.b2 = (const u16*)d_in[10];
  p.out = (u16*)d_out; p.ws = (unsigned char*)d_ws;
  fused<<<256, 512, 0, stream>>>(p);
}
```

```cpp
#include <hip/hip_runtime.h>
#include <hip/hip_bf16.h>

#define GAS __attribute__((address_space(1)))
#define LAS __attribute__((address_space(3)))
typedef unsigned short u16;
typedef unsigned v4u __attribute__((ext_vector_type(4)));
typedef unsigned v2u __attribute__((ext_vector_type(2)));
typedef float f32x16 __attribute__((ext_vector_type(16)));
typedef float f32x4 __attribute__((ext_vector_type(4)));
typedef float f32x2 __attribute__((ext_vector_type(2)));
typedef short s16x4 __attribute__((ext_vector_type(4)));
typedef short bf16x8 __attribute__((ext_vector_type(8)));
typedef __bf16 bf2 __attribute__((ext_vector_type(2)));
typedef __amdgpu_buffer_rsrc_t rsrc_t;

constexpr int S = 2048, D = 512, F = 1792;
constexpr unsigned OFF_EP = 0;
constexpr unsigned OFF_FA1 = 4096;
constexpr unsigned OFF_FD = 8192;
constexpr unsigned OFF_FB0 = 16384;
constexpr unsigned OFF_FB1 = OFF_FB0 + 32768;
constexpr unsigned OFF_XR = 262144;
constexpr unsigned OFF_XT = OFF_XR + D * S * 2;
constexpr unsigned OFF_HT = OFF_XT + D * S * 2;
constexpr unsigned OFF_ST = OFF_HT + D * S * 2;
constexpr unsigned OFF_HS = OFF_ST + 16384;
constexpr unsigned OFF_CS = OFF_HS + 32768;
constexpr unsigned OFF_W1 = OFF_CS + 16384;
constexpr unsigned OFF_W2 = OFF_W1 + D * F * 2;
constexpr unsigned OFF_HF = OFF_W2 + D * F * 2;
constexpr unsigned WS_END = OFF_HF + S * F * 2;
constexpr int SPIN_CAP = 1 << 16;

struct Params {
  const u16 *q, *k, *v, *shk, *ssk, *ssd, *w1, *b1, *w2, *b2;
  u16* out;
  unsigned char* ws;
};

__device__ __forceinline__ float asf(unsigned u) { return __builtin_bit_cast(float, u); }
__device__ __forceinline__ unsigned asu(float f) { return __builtin_bit_cast(unsigned, f); }
__device__ __forceinline__ float bflo(unsigned u) { return asf(u << 16); }
__device__ __forceinline__ float bfhi(unsigned u) { return asf(u & 0xffff0000u); }
__device__ __forceinline__ unsigned pk(float lo, float hi) {
  f32x2 f = {lo, hi};
  return __builtin_bit_cast(unsigned, __builtin_convertvector(f, bf2));
}
__device__ __forceinline__ f32x2 up2(unsigned u) { return f32x2{bflo(u), bfhi(u)}; }
__device__ __forceinline__ f32x2 fma2(f32x2 a, f32x2 b, f32x2 c) { return __builtin_elementwise_fma(a, b, c); }
__device__ __forceinline__ f32x4 mfma16(v4u a, v4u b, f32x4 c) {
  return __builtin_amdgcn_mfma_f32_16x16x32_bf16(__builtin_bit_cast(bf16x8, a), __builtin_bit_cast(bf16x8, b), c, 0, 0, 0);
}
__device__ __forceinline__ f32x16 mfma32(v4u a, v4u b, f32x16 c) {
  return __builtin_amdgcn_mfma_f32_32x32x16_bf16(__builtin_bit_cast(bf16x8, a), __builtin_bit_cast(bf16x8, b), c, 0, 0, 0);
}
__device__ __forceinline__ v2u trread(LAS unsigned char* p) {
  return __builtin_bit_cast(v2u, __builtin_amdgcn_ds_read_tr16_b64_v4i16((LAS s16x4*)p));
}
__device__ __forceinline__ v4u trfrag(LAS unsigned char* p, int second) {
  const v2u a = trread(p), b = trread(p + second);
  return v4u{a.x, a.y, b.x, b.y};
}
__device__ __forceinline__ float dot2(unsigned a, unsigned b, float c) {
  return __builtin_amdgcn_fdot2_f32_bf16(__builtin_bit_cast(bf2, a), __builtin_bit_cast(bf2, b), c, false);
}
constexpr int SC1 = 16;
#define DMA_HEAD "s_mov_b32 m0, %1\n\ts_nop 0\n\t"
#define DMA_P(o, f) "global_load_lds_dwordx4 %0, off offset:" #o f "\n\t"
__device__ __forceinline__ void dma3(const GAS void* g, unsigned lds) {
  asm volatile(DMA_HEAD DMA_P(0, "") DMA_P(1024, "") DMA_P(2048, "") :: "v"(g), "s"(lds) : "memory");
}
__device__ __forceinline__ void dma1(const GAS void* g, unsigned lds) {
  asm volatile(DMA_HEAD DMA_P(0, "") :: "v"(g), "s"(lds) : "memory");
}
__device__ __forceinline__ void dma4s(const GAS void* g, unsigned lds) {
  asm volatile(DMA_HEAD DMA_P(0, " sc1") DMA_P(1024, " sc1") DMA_P(2048, " sc1") DMA_P(3072, " sc1") :: "v"(g), "s"(lds) : "memory");
}
__device__ __forceinline__ void dma2s(const GAS void* g, unsigned lds) {
  asm volatile(DMA_HEAD DMA_P(0, " sc1") DMA_P(1024, " sc1") :: "v"(g), "s"(lds) : "memory");
}
__device__ __forceinline__ void dma1s(const GAS void* g, unsigned lds) {
  asm volatile(DMA_HEAD DMA_P(0, " sc1") :: "v"(g), "s"(lds) : "memory");
}
__device__ __forceinline__ v4u ld16(rsrc_t rs, unsigned off) { return __builtin_amdgcn_raw_buffer_load_b128(rs, off, 0, SC1); }
__device__ __forceinline__ void st16(v4u v, rsrc_t rs, unsigned off) { __builtin_amdgcn_raw_buffer_store_b128(v, rs, off, 0, SC1); }
__device__ __forceinline__ void st4(unsigned v, rsrc_t rs, unsigned off) { __builtin_amdgcn_raw_buffer_store_b32(v, rs, off, 0, SC1); }
template <int CTRL, int RM>
__device__ __forceinline__ float dppadd(float x) {
  return x + asf(__builtin_amdgcn_update_dpp(0, asu(x), CTRL, RM, 0xf, true));
}
__device__ __forceinline__ float wavesum(float x) {
  x = dppadd<0x111, 0xf>(x); x = dppadd<0x112, 0xf>(x); x = dppadd<0x114, 0xf>(x); x = dppadd<0x118, 0xf>(x);
  x = dppadd<0x142, 0xa>(x); x = dppadd<0x143, 0xc>(x);
  return x;
}
__device__ __forceinline__ float halfsum(float x) {
  const unsigned u = asu(x);
  auto r = __builtin_amdgcn_permlane32_swap(u, u, false, false);
  const unsigned r0 = r[0], r1 = r[1];
  return asf(r0) + asf(r1);
}
#define WAIT_V(n) asm volatile("s_waitcnt vmcnt(" #n ")" ::: "memory")
#define BARRIER() do { asm volatile("s_waitcnt lgkmcnt(0)" ::: "memory"); __builtin_amdgcn_s_barrier(); asm volatile("" ::: "memory"); } while (0)
#define SCHED() __builtin_amdgcn_sched_barrier(0)
constexpr unsigned ONES = 0x3f803f80u;

constexpr unsigned CODE_SPAN = 21504;
constexpr int LDS_BYTES = 163840;
constexpr int A_K = 0, A_V = A_K + 68 * 128, A_Q = A_V + 8192, A_END = A_Q + 8192;
constexpr int B_HCS = 4160, B_XCS = 4640;
constexpr int L_FLAG = LDS_BYTES - 64;
constexpr int B_XC = L_FLAG - 4 * B_XCS, B_HC = B_XC - 8 * B_HCS;
constexpr int B_RED = 0, B_ST = 65536, A_W1 = B_ST + 16384, A_W2 = A_W1 + 8192;
constexpr int D_TILE = 0, D_CSG = 131072, D_CB = D_CSG + 1024, D_END = D_CB + 1024;
constexpr int E_RED = 0, E_RES = 143360;
static_assert(A_W2 + 8192 <= B_HC && A_END <= 65536 && B_HC % 16 == 0 && D_END <= E_RES && E_RES + 8192 <= L_FLAG, "");

__device__ __forceinline__ void poll4(rsrc_t rs, unsigned off, unsigned tag) {
  for (int spins = 0; spins < SPIN_CAP; ++spins) {
    const unsigned f0 = __builtin_amdgcn_raw_buffer_load_b32(rs, off, 0, SC1), f1 = __builtin_amdgcn_raw_buffer_load_b32(rs, off + 1024, 0, SC1);
    const unsigned f2 = __builtin_amdgcn_raw_buffer_load_b32(rs, off + 2048, 0, SC1), f3 = __builtin_amdgcn_raw_buffer_load_b32(rs, off + 3072, 0, SC1);
    if (__builtin_amdgcn_ballot_w64(((f0 ^ tag) | (f1 ^ tag) | (f2 ^ tag) | (f3 ^ tag)) != 0u) == 0ull) break;
    __builtin_amdgcn_s_sleep(1);
    asm volatile("" ::: "memory");
  }
}
__device__ __forceinline__ void poll1(rsrc_t rs, unsigned off, unsigned tag) {
  for (int spins = 0; spins < SPIN_CAP; ++spins) {
    const unsigned f = __builtin_amdgcn_raw_buffer_load_b32(rs, off, 0, SC1);
    if (__builtin_amdgcn_ballot_w64(f == tag) == ~0ull) break;
    __builtin_amdgcn_s_sleep(1);
    asm volatile("" ::: "memory");
  }
}
__device__ __forceinline__ void lds_set(LAS unsigned char* L, int idx) {
  asm volatile("" ::: "memory");
  *(volatile LAS unsigned*)(L + L_FLAG + 4 * idx) = 1u;
}
__device__ __forceinline__ void lds_wait(LAS unsigned char* L, int idx) {
  for (int spins = 0; spins < (SPIN_CAP << 4); ++spins) {
    if (__builtin_amdgcn_readfirstlane(*(volatile LAS unsigned*)(L + L_FLAG + 4 * idx)) != 0) break;
    __builtin_amdgcn_s_sleep(1);
  }
  asm volatile("" ::: "memory");
}
#define GLD(dst, voff, base, imm) asm volatile("global_load_dwordx4 %0, %1, %2 offset:" #imm " sc1" : "=v"(dst) : "v"(voff), "s"(base) : "memory")
__device__ __forceinline__ void lds_count_flag(LAS unsigned char* L, int idx, unsigned n, int tid, bool act, unsigned tag, rsrc_t rs, unsigned off) {
  if ((tid & 63) == 0) {
    const unsigned old = __hip_atomic_fetch_add((LAS unsigned*)(L + L_FLAG + 4 * idx), 1u, __ATOMIC_RELAXED, __HIP_MEMORY_SCOPE_WORKGROUP);
    if (old == n - 1 && act) st4(tag, rs, off);
  }
}
__device__ __forceinline__ bool lds_count_flag8(LAS unsigned char* L, int idx, int lane, unsigned tag, rsrc_t rs, unsigned off) {
  unsigned old = 0;
  if (lane == 0) old = __hip_atomic_fetch_add((LAS unsigned*)(L + L_FLAG + 4 * idx), 1u, __ATOMIC_RELAXED, __HIP_MEMORY_SCOPE_WORKGROUP);
  const bool last = __builtin_amdgcn_readfirstlane(old) == 7;
  if (last && lane < 8) st4(tag, rs, off + lane * 4096);
  return last;
}
#define LOOK_ISSUE(off) do { const GAS char* fp_ = (const GAS char*)p.ws + (off); \
    asm volatile("global_load_dword %0, %1, off sc1" : "=v"(pf0) : "v"(fp_) : "memory"); \
    asm volatile("global_load_dword %0, %1, off offset:1024 sc1" : "=v"(pf1) : "v"(fp_) : "memory"); \
    asm volatile("global_load_dword %0, %1, off offset:2048 sc1" : "=v"(pf2) : "v"(fp_) : "memory"); \
    asm volatile("global_load_dword %0, %1, off offset:3072 sc1" : "=v"(pf3) : "v"(fp_) : "memory"); } while (0)
#define LOOK_CHECK(n, off) do { asm volatile("s_waitcnt vmcnt(" #n ")" : "+v"(pf0), "+v"(pf1), "+v"(pf2), "+v"(pf3) :: "memory"); \
    if (__builtin_amdgcn_ballot_w64(pf0 == tag && pf1 == tag && pf2 == tag && pf3 == tag) != ~0ull) poll4(rs, (off), tag); } while (0)
#define WAITF(n, x) asm volatile("s_waitcnt vmcnt(" #n ")" : "+v"(x) :: "memory")
#define ALOAD(op, dst, ptr) asm volatile(op " %0, %1, off" : "=v"(dst) : "v"(ptr) : "memory")

__global__ void __launch_bounds__(512, 2) fused(Params p) {
  __shared__ __attribute__((aligned(16))) unsigned char smem[LDS_BYTES];
  LAS unsigned char* L = (LAS unsigned char*)smem;
  const unsigned LB = (unsigned)(size_t)L;
  const int tid = threadIdx.x, lane = tid & 63, w = __builtin_amdgcn_readfirstlane(tid >> 6);
  const int bid = blockIdx.x, xcd = bid & 7, bix = bid >> 3;
  const rsrc_t rs = __builtin_amdgcn_make_buffer_rsrc((void*)p.ws, 0, (int)WS_END, 0x00020000);
  const int g = lane >> 4, i16 = lane & 15, tq = i16 >> 2, tp = i16 & 3;
  const int hh = lane >> 5, blk = g & 1;

  const int cj = xcd * 32 + bix;
  const bool d_act = bix < 28;
  const int d_mt = 4 * (xcd >> 1) + (d_act ? bix / 7 : 0), d_nt = 7 * (xcd & 1) + bix % 7;
  const int e_mt = 2 * xcd + (bix >> 4), e_j = (bix >> 3) & 1, e_dt = bix & 7;
  const int chl = w >> 2, q4 = w & 3, bch = 2 * cj + chl;

  v4u wf1[16];
  unsigned tag;
  unsigned hval, dv;
  {
    const int b = xcd, a = bix;
    const int ch = 64 * b + 16 * (w & 3) + i16, seg = 4 * (w >> 2) + g;
    const int srow = 64 * a + 8 * b + w;
    v2u shw; v4u qs;
    unsigned dm1 = 0;
    {
      const int rl = lane >> 3, sl = lane & 7;
      const int rhok = 4 + 8 * w + rl, rhov = 8 * w + rl;
      const unsigned fk = ((rhok >> 1) & 1) | (((rhok >> 3) & 1) << 1), fv = ((rhov >> 1) & 1) | (((rhov >> 3) & 1) << 1);
      const unsigned rowoff = ((64 * a + 8 * w + rl) * D + 64 * b) * 2 + 16 * (sl & 1);
      const unsigned uk = rowoff + 32 * ((sl >> 1) ^ fk), uv = rowoff + 32 * ((sl >> 1) ^ fv);
      if (w == 0 && a > 0 && lane < 32)
        dma1((const GAS char*)p.k + (((64 * a - 4 + rl) * D + 64 * b) * 2 + 16 * (sl & 1) + 32 * ((sl >> 1) ^ ((rl >> 1) & 1))), LB + A_K);
      dma1((const GAS char*)p.k + uk, LB + A_K + 512 + w * 1024);
      dma1((const GAS char*)p.v + uv, LB + A_V + w * 1024);
      ALOAD("global_load_dwordx2", shw, (const GAS char*)p.shk + (unsigned)ch * 8);
      dma1((const GAS char*)p.q + uv, LB + A_Q + w * 1024);
      asm volatile("global_load_dword %0, %1, off sc1" : "=v"(tag) : "v"((const GAS char*)p.ws + OFF_EP) : "memory");
      ALOAD("global_load_dwordx4", qs, (const GAS char*)p.q + (unsigned)(srow * D + 8 * lane) * 2);
      {
        const int hidx = (lane & 31) + 2 * q4 - 30;
        ALOAD("global_load_ushort", hval, (const GAS u16*)(p.ssk + (size_t)bch * S + (hidx < 0 ? 0 : hidx)));
        ALOAD("global_load_ushort", dv, (const GAS u16*)(p.ssd + bch));
      }
      {
        const unsigned i = (unsigned)bid * 5 + (unsigned)(lane < 5 ? lane : 0);
        const unsigned off = i < 608 ? 4096 + i * 128 : OFF_ST + (i - 608 < 512 ? i - 608 : 0) * 128;
        if (w == 7) asm volatile("global_load_dword %0, %1, off sc1" : "=v"(dm1) : "v"((const GAS char*)p.ws + off) : "memory");
      }
    }
    if (a == 0 && tid < 32) *(LAS v4u*)(L + A_K + 16 * tid) = v4u{0, 0, 0, 0};
    if (tid < 16) *(LAS unsigned*)(L + L_FLAG + 4 * tid) = 0u;
    if (tid < 128) *(LAS v4u*)(L + B_XC + (tid >> 5) * B_XCS + 4096 + 16 * (tid & 31)) = v4u{0, 0, 0, 0};
    if (w == 7) { WAIT_V(5); } else { WAIT_V(4); }
    asm volatile("" : "+v"(shw) :: "memory");
    BARRIER();
    const int s0 = 64 * a + 8 * seg;
    const int r0 = 8 * seg + tq;
    const unsigned fr0 = (r0 >> 1) & 1, sg = seg & 1;
    const unsigned ua = ((w & 3) ^ (fr0 | (sg << 1))) * 32 + 8 * tp, ub = ((w & 3) ^ (fr0 | ((sg ^ 1) << 1))) * 32 + 8 * tp;
    {
      LAS unsigned char* kb = L + A_K + r0 * 128;
      const v2u k0 = trread(kb + ua), k1 = trread(kb + 512 + ua), k2 = trread(kb + 1024 + ub);
      LAS unsigned char* vb = L + A_V + r0 * 128 + ua;
      const v2u v0 = trread(vb), v1 = trread(vb + 512);
      LAS unsigned char* qb = L + A_Q + r0 * 128 + ua;
      const v2u q0 = trread(qb), q1 = trread(qb + 512);
      const unsigned E[6] = {k0.x, k0.y, k1.x, k1.y, k2.x, k2.y};
      unsigned O[5];
#pragma unroll
      for (int i = 0; i < 5; ++i) O[i] = __builtin_amdgcn_alignbit(E[i + 1], E[i], 16);
      const unsigned shx = shw.x, shy = shw.y;
      const unsigned w32 = __builtin_amdgcn_alignbit(shy, shy, 16);
      const unsigned w10 = __builtin_amdgcn_alignbit(shx, shx, 16);
      float ks[8];
#pragma unroll
      for (int j = 0; j < 8; ++j) {
        const unsigned pa = (j & 1) ? E[(j + 1) >> 1] : O[j >> 1];
        const unsigned pb = (j & 1) ? E[(j + 3) >> 1] : O[(j + 2) >> 1];
        ks[j] = dot2(pb, w10, dot2(pa, w32, 0.f));
      }
      const f32x2 a0 = f32x2{ks[0], ks[1]} * up2(v0.x), a1 = f32x2{ks[2], ks[3]} * up2(v0.y);
      const f32x2 a2 = f32x2{ks[4], ks[5]} * up2(v1.x), a3 = f32x2{ks[6], ks[7]} * up2(v1.y);
      const v4u xr = {pk(a3.y, a3.x), pk(a2.y, a2.x), pk(a1.y, a1.x), pk(a0.y, a0.x)};
      st16(xr, rs, OFF_XR + (ch * S + (S - 8 - s0)) * 2);
      st16(v4u{q0.x, q0.y, q1.x, q1.y}, rs, OFF_XT + (ch * S + s0) * 2);
    }
    asm volatile("s_waitcnt vmcnt(2)" : "+v"(qs), "+v"(tag), "+v"(hval), "+v"(dv), "+v"(dm1) :: "memory");
    tag += 1u;
    {
      float s1 = dot2(qs.x, ONES, dot2(qs.y, ONES, 0.f)) + dot2(qs.z, ONES, dot2(qs.w, ONES, 0.f));
      float s2 = dot2(qs.x, qs.x, dot2(qs.y, qs.y, 0.f)) + dot2(qs.z, qs.z, dot2(qs.w, qs.w, 0.f));
      s1 = wavesum(s1); s2 = wavesum(s2);
      const float mean = s1 * (1.0f / D);
      float var = __builtin_fmaf(-mean, mean, s2 * (1.0f / D));
      var = var > 0.f ? var : 0.f;
      const float rstd = __builtin_amdgcn_rsqf(var + 1e-5f);
      if (lane == 63) __builtin_amdgcn_raw_buffer_store_b64(v2u{pk(-mean * rstd, rstd), tag}, rs, OFF_ST + 8 * srow, 0, SC1);
    }
    WAIT_V(1);
    BARRIER();
    if (tid == 0) st4(tag, rs, OFF_FA1 + (b * 32 + a) * 16);
    if (w >= 4) {
      const int w4 = w - 4;
#pragma unroll
      for (int cc = 0; cc < 2; ++cc) {
        const int cw = 2 * w4 + cc;
        const u16* hrow = p.ssk + (size_t)(2 * cj + (cw >> 2)) * S;
        const int idx = 8 * (4 + lane) + 2 * (cw & 3) - 30;
        const unsigned lb = LB + B_HC + cw * B_HCS + 64;
        dma3((const GAS void*)(hrow + idx), lb);
        int idx2 = idx + 3 * 512;
        idx2 = idx2 > S - 8 ? S - 8 : idx2;
        dma1((const GAS void*)(hrow + idx2), lb + 3072);
      }
    }
    if (w == 2) {
      const unsigned long long pc = __builtin_amdgcn_s_getpc();
      const unsigned co = ((unsigned)bix * 13 + (unsigned)(lane < 13 ? lane : 0)) * 64;
      asm volatile("s_mov_b32 m0, %1\n\ts_nop 0\n\tglobal_load_lds_dword %0, off" :: "v"((const GAS char*)(pc & ~63ull) + (co < CODE_SPAN ? co : 0)), "s"(LB + A_K) : "memory");
    }
    if (w == 0) poll1(rs, OFF_FA1 + (xcd * 32 + (lane & 31)) * 16, tag);
    BARRIER();
  }

  v4u wf2[28];
  unsigned b1n;
  unsigned pf0 = 0, pf1 = 0, pf2 = 0, pf3 = 0;
  const int kh = w >> 2, nq = w & 3;
  const unsigned w1vo = OFF_W1 + (d_nt * 8 + w) * 16 * 1024 + lane * 16;
  auto w1load = [&](int i) {
    const unsigned vo = w1vo + (((i >> 2) + nq) & 3) * 4096;
    if ((i & 3) == 0) GLD(wf1[i], vo, p.ws, 0);
    if ((i & 3) == 1) GLD(wf1[i], vo, p.ws, 1024);
    if ((i & 3) == 2) GLD(wf1[i], vo, p.ws, 2048);
    if ((i & 3) == 3) GLD(wf1[i], vo, p.ws, 3072);
  };
  {
    const int c = i16, al = c & 1;
    const int sa = 256 * q4 + 8 * g + 32 * (c >> 1) + 4 * al;
    v2u xt0, xt1;
    {
      const int cp = w >> 1;
      const GAS char* xs = (const GAS char*)p.ws + OFF_XR + (2 * cj + (cp >> 1)) * (S * 2) + 2 * (1 - (cp & 1)) + (w & 1) * 2048 + lane * 16;
      dma2s(xs, LB + B_XC + cp * B_XCS + (w & 1) * 2048);
    }
    asm volatile("global_load_dwordx2 %0, %1, off sc1" : "=v"(xt0) : "v"((const GAS char*)p.ws + OFF_XT + (bch * S + sa) * 2) : "memory");
    asm volatile("global_load_dwordx2 %0, %1, off offset:2048 sc1" : "=v"(xt1) : "v"((const GAS char*)p.ws + OFF_XT + (bch * S + sa) * 2) : "memory");
    dma2s((const GAS char*)p.ws + OFF_ST + w * 2048 + lane * 16, LB + B_ST + w * 2048);
    const float dch = bflo(dv);
    if (lane < 32) *(LAS u16*)(L + B_HC + w * B_HCS + 2 * lane) = ((lane & 31) + 2 * q4 - 30) >= 0 ? (u16)hval : (u16)0;
    if (w >= 4) {
      const int w4 = w - 4, wb = bid < 224 ? bid : 0;
#pragma unroll
      for (int cc = 0; cc < 2; ++cc) {
        const int wr = 16 * cc + (lane >> 2);
        dma1((const GAS char*)p.w1 + (unsigned)((32 * (wb / 14) + wr) * F + 128 * (wb % 14) + 32 * w4 + 8 * (lane & 3)) * 2, LB + A_W1 + w4 * 2048 + cc * 1024);
        dma1((const GAS char*)p.w2 + (unsigned)((32 * (wb >> 2) + wr) * D + 128 * (wb & 3) + 32 * w4 + 8 * (lane & 3)) * 2, LB + A_W2 + w4 * 2048 + cc * 1024);
      }
      WAIT_V(8);
    } else {
      WAIT_V(4);
    }
    if ((w & 3) == 1 && lane == 0) *(LAS u16*)(L + B_XC + (w >> 1) * B_XCS + 2 * (S - 1)) = 0;
    BARRIER();

    LAS unsigned char* ab = L + B_HC + (chl * 4 + (c & 3)) * B_HCS + 2 * (224 + 8 * (c >> 2) + 8 * g) - 128 * q4;
    LAS unsigned char* bb = L + B_XC + (chl * 2 + al) * B_XCS + 2 * (2240 - 1792 - 32 * (c >> 1) + 8 * g) - 128 * q4;
    LAS f32x4* red = (LAS f32x4*)(L + B_RED);
    auto bfinish = [&](int hf, v2u xt) {
      LAS f32x4* rp = red + (chl * 32 + hf * 4 + q4) * 64 + lane;
      const f32x4 a0 = rp[0], a1 = rp[8 * 64], a2 = rp[16 * 64], a3 = rp[24 * 64];
      const int s = sa + 1024 * hf;
      const v2u kvr = *(LAS v2u*)(L + B_XC + (chl * 2 + 1) * B_XCS + 2 * (S - 4 - s));
      v4u g0 = *(LAS v4u*)(L + B_ST + 8 * s), g1 = *(LAS v4u*)(L + B_ST + 8 * s + 16);
      if (__builtin_amdgcn_ballot_w64(g0.y == tag && g0.w == tag && g1.y == tag && g1.w == tag) != ~0ull) {
        for (int spins = 0; spins < SPIN_CAP; ++spins) {
          unsigned long long e[4];
#pragma unroll
          for (int i = 0; i < 4; ++i) e[i] = __hip_atomic_load((const GAS unsigned long long*)(p.ws + OFF_ST + 8 * s) + i, __ATOMIC_RELAXED, __HIP_MEMORY_SCOPE_AGENT);
          g0 = v4u{(unsigned)e[0], (unsigned)(e[0] >> 32), (unsigned)e[1], (unsigned)(e[1] >> 32)};
          g1 = v4u{(unsigned)e[2], (unsigned)(e[2] >> 32), (unsigned)e[3], (unsigned)(e[3] >> 32)};
          if (__builtin_amdgcn_ballot_w64(g0.y == tag && g0.w == tag && g1.y == tag && g1.w == tag) == ~0ull) break;
          __builtin_amdgcn_s_sleep(2);
        }
      }
      const unsigned u0 = g0.x, u1 = g0.z, u2 = g1.x, u3 = g1.z;
      const f32x2 mr01 = {bflo(u0), bflo(u1)}, rr01 = {bfhi(u0), bfhi(u1)}, mr23 = {bflo(u2), bflo(u3)}, rr23 = {bfhi(u2), bfhi(u3)};
      const f32x2 ylo = (a0.xy + a1.xy) + (a2.xy + a3.xy), yhi = (a0.zw + a1.zw) + (a2.zw + a3.zw);
      const float s0_ = al ? ylo.x : yhi.x, s1_ = al ? ylo.y : yhi.y;
      const float r0_ = asf(__builtin_amdgcn_update_dpp(0, asu(s0_), 0xB1, 0xf, 0xf, true));
      const float r1_ = asf(__builtin_amdgcn_update_dpp(0, asu(s1_), 0xB1, 0xf, 0xf, true));
      const f32x2 y01 = {al ? r0_ : ylo.x, al ? yhi.x : r0_}, y23 = {al ? r1_ : ylo.y, al ? yhi.y : r1_};
      const unsigned k1 = kvr.y, k0 = kvr.x;
      const f32x2 x01 = up2(xt.x), x23 = up2(xt.y);
      const f32x2 dd = {dch, dch};
      const f32x2 t01 = fma2(f32x2{bfhi(k1), bflo(k1)}, dd, y01), t23 = fma2(f32x2{bfhi(k0), bflo(k0)}, dd, y23);
      const f32x2 h01 = fma2(fma2(x01, rr01, mr01), t01, x01), h23 = fma2(fma2(x23, rr23, mr23), t23, x23);
      const int sl = (s & 63) >> 2;
      __builtin_amdgcn_raw_buffer_store_b64(v2u{pk(h01.x, h01.y), pk(h23.x, h23.y)}, rs,
                                            OFF_HT + ((s >> 6) * D + bch) * 128 + ((sl ^ (((bch >> 1) & 1) << 3)) << 3), 0, SC1);
    };
    {
      v4u af[8], bf[8], ag[8], bg[8];
      f32x4 acc[4];
#pragma unroll
      for (int i = 0; i < 4; ++i) acc[i] = f32x4{0.f, 0.f, 0.f, 0.f};
#pragma unroll
      for (int i = 0; i < 4; ++i) { bf[i] = *(LAS v4u*)(bb + 512 * (7 - i)); af[i] = *(LAS v4u*)(ab + 512 * i); }
#pragma unroll
      for (int i = 0; i < 4; ++i) { bg[i] = *(LAS v4u*)(bb - 64 + 512 * (7 - i)); ag[i] = *(LAS v4u*)(ab - 64 + 512 * i); }
      SCHED();
#pragma unroll
      for (int ip = 0; ip < 4; ++ip)
#pragma unroll
        for (int i = ip; i < 4; ++i) acc[i] = mfma16(af[i - ip], bf[ip], acc[i]);
#pragma unroll
      for (int i = 4; i < 8; ++i) { bf[i] = *(LAS v4u*)(bb + 512 * (7 - i)); af[i] = *(LAS v4u*)(ab + 512 * i); }
      SCHED();

#pragma unroll
      for (int i = 0; i < 4; ++i)
#pragma unroll
        for (int ip = 0; ip <= i; ++ip) acc[i] = mfma16(ag[i - ip], bg[ip], acc[i]);
      SCHED();
      asm volatile("s_waitcnt vmcnt(0)" : "+v"(xt0), "+v"(xt1) :: "memory");
      if (w >= 4 && bid < 224) {
#pragma unroll
        for (int cc = 0; cc < 2; ++cc) {
          const int v = w & 3;
          const int off = v * 2048 + (16 * cc + 8 * hh + tq) * 64 + 32 * blk + 16 * (tp & 1) + 8 * (tp >> 1);
          const v4u f1 = trfrag(L + A_W1 + off, 256);
          const v4u f2 = trfrag(L + A_W2 + off, 256);
          const int k16 = 2 * (bid / 14) + cc, nt = bid % 14;
          st16(f1, rs, OFF_W1 + (((nt * 8 + (k16 >> 4) * 4 + v) * 16) + (k16 & 15)) * 1024 + lane * 16);
          const int ka = 2 * (bid >> 2) + cc, dt = 2 * (bid & 3) + (v >> 1);
          st16(f2, rs, OFF_W2 + (((dt * 8 + ka / 14) * 28) + (ka % 14) * 2 + (v & 1)) * 1024 + lane * 16);
        }
      }
#pragma unroll
      for (int i = 0; i < 4; ++i) red[(w * 8 + i) * 64 + lane] = acc[i];
      BARRIER();
      f32x4 ac2[4];
#pragma unroll
      for (int i = 0; i < 4; ++i) ac2[i] = f32x4{0.f, 0.f, 0.f, 0.f};
      bfinish(0, xt0);
      SCHED();
#pragma unroll
      for (int ip = 0; ip < 8; ++ip) {
#pragma unroll
        for (int i = (ip > 4 ? ip : 4); i < 8; ++i) ac2[i - 4] = mfma16(af[i - ip], bf[ip], ac2[i - 4]);
        SCHED();
        if (ip < 4) { ag[4 + ip] = *(LAS v4u*)(ab - 64 + 512 * (4 + ip)); bg[4 + ip] = *(LAS v4u*)(bb - 64 + 512 * (7 - 4 - ip)); }
        SCHED();
      }
      WAIT_V(0);
      lds_count_flag8(L, 4, lane, tag, rs, OFF_FB0 + bid * 16);
      asm volatile("global_load_ushort %0, %1, %2" : "=v"(b1n) : "v"((unsigned)(128 * d_nt + 32 * nq + (lane & 31)) * 2), "s"(p.b1) : "memory");
      SCHED();
#pragma unroll
      for (int i = 4; i < 8; ++i) {
#pragma unroll
        for (int ip = 0; ip <= i; ++ip) {
          ac2[i - 4] = mfma16(ag[i - ip], bg[ip], ac2[i - 4]);
        }
        SCHED();
        if (i > 4) red[(w * 8 + 4 + i - 5) * 64 + lane] = ac2[i - 5];
        SCHED();
      }
      red[(w * 8 + 7) * 64 + lane] = ac2[3];
    }
    BARRIER();
    if (w == 0) LOOK_ISSUE(OFF_FB0 + xcd * 4096 + lane * 16);
    bfinish(1, xt1);
    if (w == 0) LOOK_CHECK(1, OFF_FB0 + xcd * 4096 + lane * 16);
  }

  {
    BARRIER();
    const GAS unsigned char* tsrc = (const GAS unsigned char*)p.ws + OFF_HT + (size_t)d_mt * 65536 + w * 8192 + lane * 16;
    const unsigned w2off = OFF_W2 + (e_dt * 8 + w) * 28 * 1024 + lane * 16;
    auto second_wait = [&]() {
      if (w == 0) { poll4(rs, OFF_FB1 + xcd * 4096 + lane * 16, tag); lds_set(L, 0); }
      lds_wait(L, 0);
    };
    auto second_res = [&]() {
      const int rr = 16 * (w & 3) + (lane >> 2);
      const int dch2 = 64 * e_dt + rr;
      dma1s((const GAS unsigned char*)p.ws + OFF_HT + ((16 * (w >> 2) + e_mt) * D + dch2) * 128 + (((unsigned)e_j ^ ((dch2 >> 1) & 1)) << 6) + 16 * (lane & 3),
            LB + E_RES + w * 1024);
    };
    if (d_act) { dma4s(tsrc, LB + D_TILE + w * 8192); dma4s(tsrc + 4096, LB + D_TILE + w * 8192 + 4096); }
    if (d_act) { WAIT_V(8); } else { WAIT_V(0); }
    lds_count_flag8(L, 5, lane, tag, rs, OFF_FB1 + bid * 16);
    if (d_act) {
      asm volatile("" : "+v"(b1n) :: "memory");
#pragma unroll
      for (int i = 0; i < 8; ++i) w1load(i);
      SCHED();
      const unsigned basel = (unsigned)(D_TILE + (256 * kh + 8 * hh + tq) * 128 + 32 * blk + 8 * tp);
      const unsigned fsw = ((tq >> 1) & 1) << 6;
      const unsigned adr[2] = {basel + ((unsigned)(kh << 6) ^ fsw), basel + ((unsigned)((kh ^ 1) << 6) ^ fsw)};
      f32x16 acc[2][2];
      auto fraddr = [&](int c, int m) { return L + (adr[m & 1] + (unsigned)(c * 65536 + 8192 * ((((m >> 1) >> 2) + nq) & 3)) + 2048 * ((m >> 1) & 3)); };
      float xg[16];
      unsigned og[8];
      auto hstore = [&](int c) {
        const unsigned ho = OFF_HF + ((32 * c + 2 * d_mt + kh) * 112 + 8 * d_nt + 2 * nq) * 1024 + lane * 16;
        st16(v4u{og[0], og[1], og[2], og[3]}, rs, ho);
        st16(v4u{og[4], og[5], og[6], og[7]}, rs, ho + 1024);
      };
      v2u sg[2][2];
      auto sload = [&](int c) {
        const GAS char* sp = (const GAS char*)p.ws + OFF_HS + (1024 * c + 64 * d_mt + 32 * kh + (lane & 31)) * 16;
        asm volatile("global_load_dwordx2 %0, %1, off sc1" : "=v"(sg[c][0]) : "v"(sp) : "memory");
        asm volatile("global_load_dwordx2 %0, %1, off offset:8 sc1" : "=v"(sg[c][1]) : "v"(sp) : "memory");
      };
      auto mstep = [&](int c) {
#pragma unroll
        for (int r = 0; r < 2; ++r)
#pragma unroll
          for (int e = 0; e < 16; ++e) acc[c][r][e] = 0.f;
        v4u fr[4];
#pragma unroll
        for (int n = 0; n < 3; ++n) fr[n] = trfrag(fraddr(c, n), 512);
        SCHED();
#pragma unroll
        for (int n = 0; n < 32; ++n) {
          const int i = n >> 1, r = n & 1;
          if (c == 0 && r == 0) {
            if (n < 16) { w1load(8 + i); WAITF(8, wf1[i]); }
            if (n == 16) {
              WAITF(7, wf1[8]);
              if (w == 0) { LOOK_CHECK(4, OFF_FB1 + xcd * 4096 + lane * 16); lds_set(L, 0); }
              lds_wait(L, 0);
            }
            if (n == 18) WAITF(7, wf1[9]);
            if (n == 20) WAITF(7, wf1[10]);
            if (n == 22) WAITF(7, wf1[11]);
            if (n == 24) WAITF(7, wf1[12]);
            if (n == 26) WAITF(7, wf1[13]);
            if (n == 28) WAITF(7, wf1[14]);
            if (n == 30) WAITF(9, wf1[15]);
          }
          if (c == 0 && n == 7 && w == 0) LOOK_ISSUE(OFF_FB1 + xcd * 4096 + lane * 16);
          if (c == 0 && n >= 16 && (n & 1) == 0) dma1s(tsrc + 16 * 65536 + ((n - 16) >> 1) * 1024, LB + D_TILE + 65536 + w * 8192 + ((n - 16) >> 1) * 1024);
          if (c == 1 && n == 1) second_res();
          if (c == 1 && n == 15) sload(1);
          if (c == 0 && n == 29) {
            sload(0);
            if (w == 0) dma1s((const GAS char*)p.ws + OFF_CS + d_nt * 1024 + lane * 16, LB + D_CSG);
          }
          if (n + 3 < 32) fr[(n + 3) & 3] = trfrag(fraddr(c, n + 3), 512);
          acc[c][r] = mfma32(wf1[i], fr[n & 3], acc[c][r]);
          if (c == 1 && (n & 1) == 0) wf2[n >> 1] = ld16(rs, w2off + (n >> 1) * 1024);
          SCHED();
        }
      };
      auto xwrite = [&](int c) {
        LAS f32x4* ex = (LAS f32x4*)(L + D_TILE + c * 32768) + w * 256 + lane;
#pragma unroll
        for (int qd = 0; qd < 4; ++qd) ex[qd * 64] = f32x4{acc[c][1][4 * qd], acc[c][1][4 * qd + 1], acc[c][1][4 * qd + 2], acc[c][1][4 * qd + 3]};
      };
      auto xpre = [&](int c) {
        LAS f32x4* ex = (LAS f32x4*)(L + D_TILE + c * 32768) + (w ^ 4) * 256 + lane;
        LAS f32x4* cb = (LAS f32x4*)(L + D_CB) + 16 * nq + 4 * hh;
        f32x4 pp[4], c0[4], c1[4];
#pragma unroll
        for (int qd = 0; qd < 4; ++qd) { pp[qd] = ex[qd * 64]; c0[qd] = cb[8 * (qd >> 1) + 2 * (qd & 1)]; c1[qd] = cb[8 * (qd >> 1) + 2 * (qd & 1) + 1]; }
        SCHED();
        unsigned u0 = sg[c][0].x, u1 = sg[c][1].x;
        if (__builtin_amdgcn_ballot_w64(((sg[c][0].y ^ tag) | (sg[c][1].y ^ tag)) != 0u) != 0ull) {
          const GAS unsigned long long* sp = (const GAS unsigned long long*)(p.ws + OFF_HS + (1024 * c + 64 * d_mt + 32 * kh + (lane & 31)) * 16);
          for (int spins = 0; spins < SPIN_CAP; ++spins) {
            const unsigned long long e0 = __hip_atomic_load(sp, __ATOMIC_RELAXED, __HIP_MEMORY_SCOPE_AGENT), e1 = __hip_atomic_load(sp + 1, __ATOMIC_RELAXED, __HIP_MEMORY_SCOPE_AGENT);
            u0 = (unsigned)e0; u1 = (unsigned)e1;
            if (__builtin_amdgcn_ballot_w64((((unsigned)(e0 >> 32) ^ tag) | ((unsigned)(e1 >> 32) ^ tag)) != 0u) == 0ull) break;
            __builtin_amdgcn_s_sleep(2);
          }
        }
        const float rstd = asf(u0), nm = asf(u1);
        const f32x2 rs2 = {rstd, rstd}, nm2 = {nm, nm};
#pragma unroll
        for (int qd = 0; qd < 4; ++qd)
#pragma unroll
          for (int hf = 0; hf < 2; ++hf) {
            const int e = 2 * qd + hf;
            const f32x4 cc = hf ? c1[qd] : c0[qd];
            const f32x2 a = f32x2{acc[c][0][2 * e], acc[c][0][2 * e + 1]} + (hf ? pp[qd].zw : pp[qd].xy);
            const f32x2 x = fma2(rs2, a, fma2(nm2, cc.xz, cc.yw));
            xg[2 * e] = x.x; xg[2 * e + 1] = x.y;
          }
      };
      auto cbtable = [&]() {
        const int n = 32 * nq + (lane & 31);
        v2u gq = *(LAS v2u*)(L + D_CSG + 8 * n);
        unsigned cs = gq.x;
        if (__builtin_amdgcn_ballot_w64(gq.y != tag) != 0ull) {
          for (int spins = 0; spins < SPIN_CAP; ++spins) {
            const unsigned long long e0 = __hip_atomic_load((const GAS unsigned long long*)(p.ws + OFF_CS) + 128 * d_nt + n, __ATOMIC_RELAXED, __HIP_MEMORY_SCOPE_AGENT);
            cs = (unsigned)e0;
            if (__builtin_amdgcn_ballot_w64((unsigned)(e0 >> 32) != tag) == 0ull) break;
            __builtin_amdgcn_s_sleep(2);
          }
        }
        *(LAS f32x2*)(L + D_CB + 8 * n) = f32x2{asf(cs), bflo(b1n)};
        asm volatile("s_waitcnt lgkmcnt(0)" ::: "memory");
      };
      auto gelu_all = [&]() {
#pragma unroll
        for (int e = 0; e < 8; ++e) {
          const f32x2 x = {xg[2 * e], xg[2 * e + 1]};
          const f32x2 pz = fma2(x * x, f32x2{-0.10294324f, -0.10294324f}, f32x2{-2.3022082f, -2.3022082f});
          const f32x2 z = x * pz;
          const f32x2 den = f32x2{__builtin_amdgcn_exp2f(z.x), __builtin_amdgcn_exp2f(z.y)} + f32x2{1.f, 1.f};
          const f32x2 y = x * f32x2{__builtin_amdgcn_rcpf(den.x), __builtin_amdgcn_rcpf(den.y)};
          og[e] = pk(y.x, y.y);
        }
      };
      WAIT_V(8);
      BARRIER();
      SCHED();
      mstep(0);
      BARRIER();
      xwrite(0);
      asm volatile("s_waitcnt vmcnt(0)" : "+v"(sg[0][0]), "+v"(sg[0][1]) :: "memory");
      BARRIER();
      cbtable();
      xpre(0);
      gelu_all();
      hstore(0);
      BARRIER();
      mstep(1);
      xwrite(1);
      asm volatile("s_waitcnt vmcnt(8)" : "+v"(sg[1][0]), "+v"(sg[1][1]) :: "memory");
      lds_count_flag(L, 2, 8, tid, d_act, tag, rs, OFF_FD + d_mt * 256 + d_nt * 16);
      BARRIER();
      xpre(1);
      gelu_all();
      hstore(1);
    }
    asm volatile("" ::: "memory");
    if (!d_act) {
      const int u = xcd * 4 + (bix - 28);
      if (u >= 16) {
        const int kg = tid >> 4, cc = tid & 15;
        f32x2 cs[4] = {{0.f, 0.f}, {0.f, 0.f}, {0.f, 0.f}, {0.f, 0.f}};
        if (cc < 14) {
          v4u wv[16];
#pragma unroll
          for (int i = 0; i < 16; ++i) wv[i] = *(const GAS v4u*)((const GAS char*)p.w1 + (unsigned)((16 * kg + i) * F + 112 * (u - 16) + 8 * cc) * 2);
#pragma unroll
          for (int i = 0; i < 16; ++i) { cs[0] += up2(wv[i].x); cs[1] += up2(wv[i].y); cs[2] += up2(wv[i].z); cs[3] += up2(wv[i].w); }
        }
        LAS f32x4* t = (LAS f32x4*)(L + 65536) + (kg * 16 + cc) * 2;
        t[0] = f32x4{cs[0].x, cs[0].y, cs[1].x, cs[1].y}; t[1] = f32x4{cs[2].x, cs[2].y, cs[3].x, cs[3].y};
        BARRIER();
        if (tid < 112) {
          float a = 0.f;
#pragma unroll
          for (int i = 0; i < 32; ++i) a += *(LAS float*)(L + 65536 + (i * 128 + tid) * 4);
          __builtin_amdgcn_raw_buffer_store_b64(v2u{asu(a), tag}, rs, OFF_CS + 8 * (112 * (u - 16) + tid), 0, SC1);
        }
      }
      if (u >= 16) second_wait();
      {
        const GAS unsigned char* src = (const GAS unsigned char*)p.ws + OFF_HT + (size_t)u * 65536 + w * 8192 + lane * 16;
        dma4s(src, LB + w * 8192); dma4s(src + 4096, LB + w * 8192 + 4096);
      }
      WAIT_V(0);
      {
        float s1[2][2] = {{0.f, 0.f}, {0.f, 0.f}}, s2[2][2] = {{0.f, 0.f}, {0.f, 0.f}};
        LAS unsigned char* sp = L + w * 8192 + (8 * hh + tq) * 128 + 32 * blk + 8 * tp;
        const unsigned fsw = ((tq >> 1) & 1) << 6;
#pragma unroll
        for (int n = 0; n < 8; ++n) {
          const int r = n & 1;
          const v4u hf = trfrag(sp + (n >> 1) * 2048 + (((unsigned)r << 6) ^ fsw), 512);
          s1[r][0] = dot2(hf.x, ONES, s1[r][0]); s2[r][0] = dot2(hf.x, hf.x, s2[r][0]);
          s1[r][1] = dot2(hf.y, ONES, s1[r][1]); s2[r][1] = dot2(hf.y, hf.y, s2[r][1]);
          s1[r][0] = dot2(hf.z, ONES, s1[r][0]); s2[r][0] = dot2(hf.z, hf.z, s2[r][0]);
          s1[r][1] = dot2(hf.w, ONES, s1[r][1]); s2[r][1] = dot2(hf.w, hf.w, s2[r][1]);
        }
#pragma unroll
        for (int r = 0; r < 2; ++r) {
          const float a = halfsum(s1[r][0] + s1[r][1]), b = halfsum(s2[r][0] + s2[r][1]);
          if (lane < 32) *(LAS f32x2*)(L + 98304 + (w * 64 + 32 * r + lane) * 8) = f32x2{a, b};
        }
      }
      BARRIER();
      if (w == 0) {
        f32x2 sa = {0.f, 0.f};
#pragma unroll
        for (int ww = 0; ww < 8; ++ww) sa += *(LAS f32x2*)(L + 98304 + (ww * 64 + lane) * 8);
        const float mean = sa.x * (1.0f / D);
        float var = __builtin_fmaf(-mean, mean, sa.y * (1.0f / D));
        var = var > 0.f ? var : 0.f;
        const float r1 = __builtin_amdgcn_rsqf(var + 1e-5f);
        const float rstd = r1 * __builtin_amdgcn_rsqf(__builtin_fmaf(var * r1, r1, 1e-5f));
        __builtin_amdgcn_raw_buffer_store_b64(v2u{asu(rstd), tag}, rs, OFF_HS + (64 * u + lane) * 16, 0, SC1);
        __builtin_amdgcn_raw_buffer_store_b64(v2u{asu(-rstd * mean), tag}, rs, OFF_HS + (64 * u + lane) * 16 + 8, 0, SC1);
      }
      if (u < 16) second_wait();
      second_res();
#pragma unroll
      for (int i = 0; i < 16; ++i) wf2[i] = ld16(rs, w2off + i * 1024);
    }
  }

  {
    const int sbo = w >> 2, dbo = (w >> 1) & 1, hf = w & 1;
    if (w == 0) poll1(rs, OFF_FD + e_mt * 256 + (lane < 14 ? lane : 0) * 16, tag);
    const unsigned w2off = OFF_W2 + (e_dt * 8 + w) * 28 * 1024 + lane * 16;
#pragma unroll
    for (int i = 16; i < 28; ++i) wf2[i] = ld16(rs, w2off + i * 1024);
    const v4u b2v = *(const GAS v4u*)((const GAS char*)p.b2 + (unsigned)(64 * e_dt + 32 * dbo + 16 * hf + 8 * hh) * 2);
    SCHED();
    WAIT_V(13);
    BARRIER();
    if (d_act && tid == 0) st4(tag, rs, OFF_FD + 4096 + d_mt * 256 + d_nt * 16);
    const unsigned hoff0 = OFF_HF + ((2 * e_mt + e_j) * 112 + 14 * w) * 1024 + lane * 16, hoff1 = hoff0 + 32 * 112 * 1024;
    if (w == 0) { poll1(rs, OFF_FD + 4096 + e_mt * 256 + (lane < 14 ? lane : 0) * 16, tag); lds_set(L, 1); }
    v4u hf0[14], hf1[14];
#pragma unroll
    for (int k = 0; k < 14; ++k) hf0[k] = ld16(rs, hoff0 + k * 1024);
    SCHED();
    lds_wait(L, 1);
    LAS f32x4* red = (LAS f32x4*)(L + E_RED + w * 16384) + lane;
    {
      f32x16 acc[2];
#pragma unroll
      for (int e = 0; e < 16; ++e) { acc[0][e] = 0.f; acc[1][e] = 0.f; }
#pragma unroll
      for (int k = 0; k < 14; ++k) {
        acc[0] = mfma32(wf2[2 * k], hf0[k], acc[0]); acc[1] = mfma32(wf2[2 * k + 1], hf0[k], acc[1]);
        SCHED();
        hf1[k] = ld16(rs, hoff1 + k * 1024);
        SCHED();
      }
#pragma unroll
      for (int d = 0; d < 2; ++d)
#pragma unroll
        for (int qd = 0; qd < 4; ++qd) red[(d * 4 + qd) * 64] = f32x4{acc[d][4 * qd], acc[d][4 * qd + 1], acc[d][4 * qd + 2], acc[d][4 * qd + 3]};
    }
    {
      f32x16 acc[2];
#pragma unroll
      for (int e = 0; e < 16; ++e) { acc[0][e] = 0.f; acc[1][e] = 0.f; }
#pragma unroll
      for (int k = 0; k < 14; ++k) { acc[0] = mfma32(wf2[2 * k], hf1[k], acc[0]); acc[1] = mfma32(wf2[2 * k + 1], hf1[k], acc[1]); }
#pragma unroll
      for (int d = 0; d < 2; ++d)
#pragma unroll
        for (int qd = 0; qd < 4; ++qd) red[512 + (d * 4 + qd) * 64] = f32x4{acc[d][4 * qd], acc[d][4 * qd + 1], acc[d][4 * qd + 2], acc[d][4 * qd + 3]};
    }
    BARRIER();
    f32x2 o[4] = {{0.f, 0.f}, {0.f, 0.f}, {0.f, 0.f}, {0.f, 0.f}};
#pragma unroll
    for (int ww = 0; ww < 8; ++ww) {
      LAS f32x4* rp = (LAS f32x4*)(L + E_RED + ww * 16384) + sbo * 512 + (dbo * 4 + 2 * hf) * 64 + lane;
      const f32x4 a = rp[0], b = rp[64];
      o[0] += a.xy; o[1] += a.zw; o[2] += b.xy; o[3] += b.zw;
    }
    const v4u hr = trfrag(L + E_RES + sbo * 4096 + (32 * dbo + 16 * hf + 8 * hh + tq) * 64 + 32 * blk + 8 * tp, 256);
    o[0] += up2(b2v.x) + up2(hr.x); o[1] += up2(b2v.y) + up2(hr.y);
    o[2] += up2(b2v.z) + up2(hr.z); o[3] += up2(b2v.w) + up2(hr.w);
    const v4u ov = {pk(o[0].x, o[0].y), pk(o[1].x, o[1].y), pk(o[2].x, o[2].y), pk(o[3].x, o[3].y)};
    {
      const rsrc_t ro = __builtin_amdgcn_make_buffer_rsrc((void*)p.out, 0, S * D * 2, 0x00020000);
      __builtin_amdgcn_raw_buffer_store_b128(ov, ro, (unsigned)((1024 * sbo + 64 * e_mt + 32 * e_j + (lane & 31)) * D + 64 * e_dt + 32 * dbo + 16 * hf + 8 * hh) * 2, 0, 0);
    }
    if (bid == 0 && tid == 0) st4(tag, rs, OFF_EP);
  }
}

extern "C" void kernel_launch(void* const* d_in, const int* in_sizes, int n_in, void* d_out, int out_size, void* d_ws,
                              size_t ws_size, hipStream_t stream) {
  Params p;
  p.q = (const u16*)d_in[0]; p.k = (const u16*)d_in[1]; p.v = (const u16*)d_in[2];
  p.shk = (const u16*)d_in[3]; p.ssk = (const u16*)d_in[4]; p.ssd = (const u16*)d_in[5];
  p.w1 = (const u16*)d_in[7]; p.b1 = (const u16*)d_in[8]; p.w2 = (const u16*)d_in[9]; p.b2 = (const u16*)d_in[10];
  p.out = (u16*)d_out; p.ws = (unsigned char*)d_ws;
  fused<<<256, 512, 0, stream>>>(p);
}
```

```cpp
#include <hip/hip_runtime.h>
#include <hip/hip_bf16.h>

#define GAS __attribute__((address_space(1)))
#define LAS __attribute__((address_space(3)))
typedef unsigned short u16;
typedef unsigned v4u __attribute__((ext_vector_type(4)));
typedef unsigned v2u __attribute__((ext_vector_type(2)));
typedef float f32x16 __attribute__((ext_vector_type(16)));
typedef float f32x4 __attribute__((ext_vector_type(4)));
typedef float f32x2 __attribute__((ext_vector_type(2)));
typedef short s16x4 __attribute__((ext_vector_type(4)));
typedef short bf16x8 __attribute__((ext_vector_type(8)));
typedef __bf16 bf2 __attribute__((ext_vector_type(2)));
typedef __amdgpu_buffer_rsrc_t rsrc_t;

constexpr int S = 2048, D = 512, F = 1792;
constexpr unsigned OFF_EP = 0;
constexpr unsigned OFF_FA1 = 4096;
constexpr unsigned OFF_FD = 8192;
constexpr unsigned OFF_FB0 = 16384;
constexpr unsigned OFF_FB1 = OFF_FB0 + 32768;
constexpr unsigned OFF_XR = 262144;
constexpr unsigned OFF_XT = OFF_XR + D * S * 2;
constexpr unsigned OFF_HT = OFF_XT + D * S * 2;
constexpr unsigned OFF_ST = OFF_HT + D * S * 2;
constexpr unsigned OFF_HS = OFF_ST + 16384;
constexpr unsigned OFF_CS = OFF_HS + 32768;
constexpr unsigned OFF_W1 = OFF_CS + 16384;
constexpr unsigned OFF_W2 = OFF_W1 + D * F * 2;
constexpr unsigned OFF_HF = OFF_W2 + D * F * 2;
constexpr unsigned WS_END = OFF_HF + S * F * 2;
constexpr int SPIN_CAP = 1 << 16;

struct Params {
  const u16 *q, *k, *v, *shk, *ssk, *ssd, *w1, *b1, *w2, *b2;
  u16* out;
  unsigned char* ws;
};

__device__ __forceinline__ float asf(unsigned u) { return __builtin_bit_cast(float, u); }
__device__ __forceinline__ unsigned asu(float f) { return __builtin_bit_cast(unsigned, f); }
__device__ __forceinline__ float bflo(unsigned u) { return asf(u << 16); }
__device__ __forceinline__ float bfhi(unsigned u) { return asf(u & 0xffff0000u); }
__device__ __forceinline__ unsigned pk(float lo, float hi) {
  f32x2 f = {lo, hi};
  return __builtin_bit_cast(unsigned, __builtin_convertvector(f, bf2));
}
__device__ __forceinline__ f32x2 up2(unsigned u) { return f32x2{bflo(u), bfhi(u)}; }
__device__ __forceinline__ f32x2 fma2(f32x2 a, f32x2 b, f32x2 c) { return __builtin_elementwise_fma(a, b, c); }
__device__ __forceinline__ f32x4 mfma16(v4u a, v4u b, f32x4 c) {
  return __builtin_amdgcn_mfma_f32_16x16x32_bf16(__builtin_bit_cast(bf16x8, a), __builtin_bit_cast(bf16x8, b), c, 0, 0, 0);
}
__device__ __forceinline__ f32x16 mfma32(v4u a, v4u b, f32x16 c) {
  return __builtin_amdgcn_mfma_f32_32x32x16_bf16(__builtin_bit_cast(bf16x8, a), __builtin_bit_cast(bf16x8, b), c, 0, 0, 0);
}
__device__ __forceinline__ v2u trread(LAS unsigned char* p) {
  return __builtin_bit_cast(v2u, __builtin_amdgcn_ds_read_tr16_b64_v4i16((LAS s16x4*)p));
}
__device__ __forceinline__ v4u trfrag(LAS unsigned char* p, int second) {
  const v2u a = trread(p), b = trread(p + second);
  return v4u{a.x, a.y, b.x, b.y};
}
__device__ __forceinline__ float dot2(unsigned a, unsigned b, float c) {
  return __builtin_amdgcn_fdot2_f32_bf16(__builtin_bit_cast(bf2, a), __builtin_bit_cast(bf2, b), c, false);
}
constexpr int SC1 = 16;
#define DMA_HEAD "s_mov_b32 m0, %1\n\ts_nop 0\n\t"
#define DMA_P(o, f) "global_load_lds_dwordx4 %0, off offset:" #o f "\n\t"
__device__ __forceinline__ void dma3(const GAS void* g, unsigned lds) {
  asm volatile(DMA_HEAD DMA_P(0, "") DMA_P(1024, "") DMA_P(2048, "") :: "v"(g), "s"(lds) : "memory");
}
__device__ __forceinline__ void dma1(const GAS void* g, unsigned lds) {
  asm volatile(DMA_HEAD DMA_P(0, "") :: "v"(g), "s"(lds) : "memory");
}
__device__ __forceinline__ void dma4s(const GAS void* g, unsigned lds) {
  asm volatile(DMA_HEAD DMA_P(0, " sc1") DMA_P(1024, " sc1") DMA_P(2048, " sc1") DMA_P(3072, " sc1") :: "v"(g), "s"(lds) : "memory");
}
__device__ __forceinline__ void dma2s(const GAS void* g, unsigned lds) {
  asm volatile(DMA_HEAD DMA_P(0, " sc1") DMA_P(1024, " sc1") :: "v"(g), "s"(lds) : "memory");
}
__device__ __forceinline__ void dma1s(const GAS void* g, unsigned lds) {
  asm volatile(DMA_HEAD DMA_P(0, " sc1") :: "v"(g), "s"(lds) : "memory");
}
__device__ __forceinline__ v4u ld16(rsrc_t rs, unsigned off) { return __builtin_amdgcn_raw_buffer_load_b128(rs, off, 0, SC1); }
__device__ __forceinline__ void st16(v4u v, rsrc_t rs, unsigned off) { __builtin_amdgcn_raw_buffer_store_b128(v, rs, off, 0, SC1); }
__device__ __forceinline__ void st4(unsigned v, rsrc_t rs, unsigned off) { __builtin_amdgcn_raw_buffer_store_b32(v, rs, off, 0, SC1); }
template <int CTRL, int RM>
__device__ __forceinline__ float dppadd(float x) {
  return x + asf(__builtin_amdgcn_update_dpp(0, asu(x), CTRL, RM, 0xf, true));
}
__device__ __forceinline__ float wavesum(float x) {
  x = dppadd<0x111, 0xf>(x); x = dppadd<0x112, 0xf>(x); x = dppadd<0x114, 0xf>(x); x = dppadd<0x118, 0xf>(x);
  x = dppadd<0x142, 0xa>(x); x = dppadd<0x143, 0xc>(x);
  return x;
}
__device__ __forceinline__ float halfsum(float x) {
  const unsigned u = asu(x);
  auto r = __builtin_amdgcn_permlane32_swap(u, u, false, false);
  const unsigned r0 = r[0], r1 = r[1];
  return asf(r0) + asf(r1);
}
#define WAIT_V(n) asm volatile("s_waitcnt vmcnt(" #n ")" ::: "memory")
#define BARRIER() do { asm volatile("s_waitcnt lgkmcnt(0)" ::: "memory"); __builtin_amdgcn_s_barrier(); asm volatile("" ::: "memory"); } while (0)
#define SCHED() __builtin_amdgcn_sched_barrier(0)
constexpr unsigned ONES = 0x3f803f80u;

constexpr unsigned CODE_SPAN = 20480;
constexpr int LDS_BYTES = 163840;
constexpr int A_K = 0, A_V = A_K + 68 * 128, A_Q = A_V + 8192, A_END = A_Q + 8192;
constexpr int B_HCS = 4160, B_XCS = 4640;
constexpr int L_FLAG = LDS_BYTES - 64;
constexpr int B_XC = L_FLAG - 4 * B_XCS, B_HC = B_XC - 8 * B_HCS;
constexpr int B_RED = 0, B_ST = 65536, A_W1 = B_ST + 16384, A_W2 = A_W1 + 8192;
constexpr int D_TILE = 0, D_CSG = 131072, D_CB = D_CSG + 1024, D_END = D_CB + 1024;
constexpr int E_RED = 0, E_RES = 143360;
static_assert(A_W2 + 8192 <= B_HC && A_END <= 65536 && B_HC % 16 == 0 && D_END <= E_RES && E_RES + 8192 <= L_FLAG, "");

__device__ __forceinline__ void poll4(rsrc_t rs, unsigned off, unsigned tag) {
  for (int spins = 0; spins < SPIN_CAP; ++spins) {
    const unsigned f0 = __builtin_amdgcn_raw_buffer_load_b32(rs, off, 0, SC1), f1 = __builtin_amdgcn_raw_buffer_load_b32(rs, off + 1024, 0, SC1);
    const unsigned f2 = __builtin_amdgcn_raw_buffer_load_b32(rs, off + 2048, 0, SC1), f3 = __builtin_amdgcn_raw_buffer_load_b32(rs, off + 3072, 0, SC1);
    if (__builtin_amdgcn_ballot_w64(((f0 ^ tag) | (f1 ^ tag) | (f2 ^ tag) | (f3 ^ tag)) != 0u) == 0ull) break;
    __builtin_amdgcn_s_sleep(1);
    asm volatile("" ::: "memory");
  }
}
__device__ __forceinline__ void poll1(rsrc_t rs, unsigned off, unsigned tag) {
  for (int spins = 0; spins < SPIN_CAP; ++spins) {
    const unsigned f = __builtin_amdgcn_raw_buffer_load_b32(rs, off, 0, SC1);
    if (__builtin_amdgcn_ballot_w64(f == tag) == ~0ull) break;
    __builtin_amdgcn_s_sleep(1);
    asm volatile("" ::: "memory");
  }
}
__device__ __forceinline__ void lds_set(LAS unsigned char* L, int idx) {
  asm volatile("" ::: "memory");
  *(volatile LAS unsigned*)(L + L_FLAG + 4 * idx) = 1u;
}
__device__ __forceinline__ void lds_wait(LAS unsigned char* L, int idx) {
  for (int spins = 0; spins < (SPIN_CAP << 4); ++spins) {
    if (__builtin_amdgcn_readfirstlane(*(volatile LAS unsigned*)(L + L_FLAG + 4 * idx)) != 0) break;
    __builtin_amdgcn_s_sleep(1);
  }
  asm volatile("" ::: "memory");
}
#define GLD(dst, voff, base, imm) asm volatile("global_load_dwordx4 %0, %1, %2 offset:" #imm " sc1" : "=v"(dst) : "v"(voff), "s"(base) : "memory")
__device__ __forceinline__ void lds_count_flag(LAS unsigned char* L, int idx, unsigned n, int tid, bool act, unsigned tag, rsrc_t rs, unsigned off) {
  if ((tid & 63) == 0) {
    const unsigned old = __hip_atomic_fetch_add((LAS unsigned*)(L + L_FLAG + 4 * idx), 1u, __ATOMIC_RELAXED, __HIP_MEMORY_SCOPE_WORKGROUP);
    if (old == n - 1 && act) st4(tag, rs, off);
  }
}
__device__ __forceinline__ bool lds_count_flag8(LAS unsigned char* L, int idx, int lane, unsigned tag, rsrc_t rs, unsigned off) {
  unsigned old = 0;
  if (lane == 0) old = __hip_atomic_fetch_add((LAS unsigned*)(L + L_FLAG + 4 * idx), 1u, __ATOMIC_RELAXED, __HIP_MEMORY_SCOPE_WORKGROUP);
  const bool last = __builtin_amdgcn_readfirstlane(old) == 7;
  if (last && lane < 8) st4(tag, rs, off + lane * 4096);
  return last;
}
#define LOOK_ISSUE(off) do { const GAS char* fp_ = (const GAS char*)p.ws + (off); \
    asm volatile("global_load_dword %0, %1, off sc1" : "=v"(pf0) : "v"(fp_) : "memory"); \
    asm volatile("global_load_dword %0, %1, off offset:1024 sc1" : "=v"(pf1) : "v"(fp_) : "memory"); \
    asm volatile("global_load_dword %0, %1, off offset:2048 sc1" : "=v"(pf2) : "v"(fp_) : "memory"); \
    asm volatile("global_load_dword %0, %1, off offset:3072 sc1" : "=v"(pf3) : "v"(fp_) : "memory"); } while (0)
#define LOOK_CHECK(n, off) do { asm volatile("s_waitcnt vmcnt(" #n ")" : "+v"(pf0), "+v"(pf1), "+v"(pf2), "+v"(pf3) :: "memory"); \
    if (__builtin_amdgcn_ballot_w64(pf0 == tag && pf1 == tag && pf2 == tag && pf3 == tag) != ~0ull) poll4(rs, (off), tag); } while (0)
#define WAITF(n, x) asm volatile("s_waitcnt vmcnt(" #n ")" : "+v"(x) :: "memory")
#define ALOAD(op, dst, ptr) asm volatile(op " %0, %1, off" : "=v"(dst) : "v"(ptr) : "memory")

__global__ void __launch_bounds__(512, 2) fused(Params p) {
  __shared__ __attribute__((aligned(16))) unsigned char smem[LDS_BYTES];
  LAS unsigned char* L = (LAS unsigned char*)smem;
  const unsigned LB = (unsigned)(size_t)L;
  const int tid = threadIdx.x, lane = tid & 63, w = __builtin_amdgcn_readfirstlane(tid >> 6);
  const int bid = blockIdx.x, xcd = bid & 7, bix = bid >> 3;
  const rsrc_t rs = __builtin_amdgcn_make_buffer_rsrc((void*)p.ws, 0, (int)WS_END, 0x00020000);
  const int g = lane >> 4, i16 = lane & 15, tq = i16 >> 2, tp = i16 & 3;
  const int hh = lane >> 5, blk = g & 1;

  const int cj = xcd * 32 + bix;
  const bool d_act = bix < 28;
  const int d_mt = 4 * (xcd >> 1) + (d_act ? bix / 7 : 0), d_nt = 7 * (xcd & 1) + bix % 7;
  const int e_mt = 2 * xcd + (bix >> 4), e_j = (bix >> 3) & 1, e_dt = bix & 7;
  const int chl = w >> 2, q4 = w & 3, bch = 2 * cj + chl;

  v4u wf1[16];
  unsigned tag;
  unsigned hval, dv;
  {
    const int b = xcd, a = bix;
    const int ch = 64 * b + 16 * (w & 3) + i16, seg = 4 * (w >> 2) + g;
    const int srow = 64 * a + 8 * b + w;
    v2u shw; v4u qs;
    unsigned dm1 = 0;
    {
      const int rl = lane >> 3, sl = lane & 7;
      const int rhok = 4 + 8 * w + rl, rhov = 8 * w + rl;
      const unsigned fk = ((rhok >> 1) & 1) | (((rhok >> 3) & 1) << 1), fv = ((rhov >> 1) & 1) | (((rhov >> 3) & 1) << 1);
      const unsigned rowoff = ((64 * a + 8 * w + rl) * D + 64 * b) * 2 + 16 * (sl & 1);
      const unsigned uk = rowoff + 32 * ((sl >> 1) ^ fk), uv = rowoff + 32 * ((sl >> 1) ^ fv);
      if (w == 0 && a > 0 && lane < 32)
        dma1((const GAS char*)p.k + (((64 * a - 4 + rl) * D + 64 * b) * 2 + 16 * (sl & 1) + 32 * ((sl >> 1) ^ ((rl >> 1) & 1))), LB + A_K);
      dma1((const GAS char*)p.k + uk, LB + A_K + 512 + w * 1024);
      dma1((const GAS char*)p.v + uv, LB + A_V + w * 1024);
      ALOAD("global_load_dwordx2", shw, (const GAS char*)p.shk + (unsigned)ch * 8);
      dma1((const GAS char*)p.q + uv, LB + A_Q + w * 1024);
      asm volatile("global_load_dword %0, %1, off sc1" : "=v"(tag) : "v"((const GAS char*)p.ws + OFF_EP) : "memory");
      ALOAD("global_load_dwordx4", qs, (const GAS char*)p.q + (unsigned)(srow * D + 8 * lane) * 2);
      {
        const int hidx = (lane & 31) + 2 * q4 - 30;
        ALOAD("global_load_ushort", hval, (const GAS u16*)(p.ssk + (size_t)bch * S + (hidx < 0 ? 0 : hidx)));
        ALOAD("global_load_ushort", dv, (const GAS u16*)(p.ssd + bch));
      }
      {
        const unsigned i = (unsigned)bid * 5 + (unsigned)(lane < 5 ? lane : 0);
        const unsigned off = i < 608 ? 4096 + i * 128 : OFF_ST + (i - 608 < 512 ? i - 608 : 0) * 128;
        if (w == 7) asm volatile("global_load_dword %0, %1, off sc1" : "=v"(dm1) : "v"((const GAS char*)p.ws + off) : "memory");
      }
    }
    if (a == 0 && tid < 32) *(LAS v4u*)(L + A_K + 16 * tid) = v4u{0, 0, 0, 0};
    if (tid < 16) *(LAS unsigned*)(L + L_FLAG + 4 * tid) = 0u;
    if (tid < 128) *(LAS v4u*)(L + B_XC + (tid >> 5) * B_XCS + 4096 + 16 * (tid & 31)) = v4u{0, 0, 0, 0};
    if (w == 7) { WAIT_V(5); } else { WAIT_V(4); }
    asm volatile("" : "+v"(shw) :: "memory");
    BARRIER();
    const int s0 = 64 * a + 8 * seg;
    const int r0 = 8 * seg + tq;
    const unsigned fr0 = (r0 >> 1) & 1, sg = seg & 1;
    const unsigned ua = ((w & 3) ^ (fr0 | (sg << 1))) * 32 + 8 * tp, ub = ((w & 3) ^ (fr0 | ((sg ^ 1) << 1))) * 32 + 8 * tp;
    {
      LAS unsigned char* kb = L + A_K + r0 * 128;
      const v2u k0 = trread(kb + ua), k1 = trread(kb + 512 + ua), k2 = trread(kb + 1024 + ub);
      LAS unsigned char* vb = L + A_V + r0 * 128 + ua;
      const v2u v0 = trread(vb), v1 = trread(vb + 512);
      LAS unsigned char* qb = L + A_Q + r0 * 128 + ua;
      const v2u q0 = trread(qb), q1 = trread(qb + 512);
      const unsigned E[6] = {k0.x, k0.y, k1.x, k1.y, k2.x, k2.y};
      unsigned O[5];
#pragma unroll
      for (int i = 0; i < 5; ++i) O[i] = __builtin_amdgcn_alignbit(E[i + 1], E[i], 16);
      const unsigned shx = shw.x, shy = shw.y;
      const unsigned w32 = __builtin_amdgcn_alignbit(shy, shy, 16);
      const unsigned w10 = __builtin_amdgcn_alignbit(shx, shx, 16);
      float ks[8];
#pragma unroll
      for (int j = 0; j < 8; ++j) {
        const unsigned pa = (j & 1) ? E[(j + 1) >> 1] : O[j >> 1];
        const unsigned pb = (j & 1) ? E[(j + 3) >> 1] : O[(j + 2) >> 1];
        ks[j] = dot2(pb, w10, dot2(pa, w32, 0.f));
      }
      const f32x2 a0 = f32x2{ks[0], ks[1]} * up2(v0.x), a1 = f32x2{ks[2], ks[3]} * up2(v0.y);
      const f32x2 a2 = f32x2{ks[4], ks[5]} * up2(v1.x), a3 = f32x2{ks[6], ks[7]} * up2(v1.y);
      const v4u xr = {pk(a3.y, a3.x), pk(a2.y, a2.x), pk(a1.y, a1.x), pk(a0.y, a0.x)};
      st16(xr, rs, OFF_XR + (ch * S + (S - 8 - s0)) * 2);
      st16(v4u{q0.x, q0.y, q1.x, q1.y}, rs, OFF_XT + (ch * S + s0) * 2);
    }
    asm volatile("s_waitcnt vmcnt(2)" : "+v"(qs), "+v"(tag), "+v"(hval), "+v"(dv), "+v"(dm1) :: "memory");
    tag += 1u;
    {
      float s1 = dot2(qs.x, ONES, dot2(qs.y, ONES, 0.f)) + dot2(qs.z, ONES, dot2(qs.w, ONES, 0.f));
      float s2 = dot2(qs.x, qs.x, dot2(qs.y, qs.y, 0.f)) + dot2(qs.z, qs.z, dot2(qs.w, qs.w, 0.f));
      s1 = wavesum(s1); s2 = wavesum(s2);
      const float mean = s1 * (1.0f / D);
      float var = __builtin_fmaf(-mean, mean, s2 * (1.0f / D));
      var = var > 0.f ? var : 0.f;
      const float rstd = __builtin_amdgcn_rsqf(var + 1e-5f);
      if (lane == 63) __builtin_amdgcn_raw_buffer_store_b64(v2u{pk(mean, rstd), tag}, rs, OFF_ST + 8 * srow, 0, SC1);
    }
    WAIT_V(1);
    BARRIER();
    if (tid == 0) st4(tag, rs, OFF_FA1 + (b * 32 + a) * 16);
    if (w >= 4) {
      const int w4 = w - 4;
#pragma unroll
      for (int cc = 0; cc < 2; ++cc) {
        const int cw = 2 * w4 + cc;
        const u16* hrow = p.ssk + (size_t)(2 * cj + (cw >> 2)) * S;
        const int idx = 8 * (4 + lane) + 2 * (cw & 3) - 30;
        const unsigned lb = LB + B_HC + cw * B_HCS + 64;
        dma3((const GAS void*)(hrow + idx), lb);
        int idx2 = idx + 3 * 512;
        idx2 = idx2 > S - 8 ? S - 8 : idx2;
        dma1((const GAS void*)(hrow + idx2), lb + 3072);
      }
    }
    if (w == 2) {
      const unsigned long long pc = __builtin_amdgcn_s_getpc();
      const unsigned co = ((unsigned)bix * 13 + (unsigned)(lane < 13 ? lane : 0)) * 64;
      asm volatile("s_mov_b32 m0, %1\n\ts_nop 0\n\tglobal_load_lds_dword %0, off" :: "v"((const GAS char*)(pc & ~63ull) + (co < CODE_SPAN ? co : 0)), "s"(LB + A_K) : "memory");
    }
    if (w == 0) poll1(rs, OFF_FA1 + (xcd * 32 + (lane & 31)) * 16, tag);
    BARRIER();
  }

  v4u wf2[28];
  unsigned b1n;
  unsigned pf0 = 0, pf1 = 0, pf2 = 0, pf3 = 0;
  const int kh = w >> 2, nq = w & 3;
  const unsigned w1vo = OFF_W1 + (d_nt * 8 + w) * 16 * 1024 + lane * 16;
  auto w1load = [&](int i) {
    const unsigned vo = w1vo + (((i >> 2) + nq) & 3) * 4096;
    if ((i & 3) == 0) GLD(wf1[i], vo, p.ws, 0);
    if ((i & 3) == 1) GLD(wf1[i], vo, p.ws, 1024);
    if ((i & 3) == 2) GLD(wf1[i], vo, p.ws, 2048);
    if ((i & 3) == 3) GLD(wf1[i], vo, p.ws, 3072);
  };
  {
    const int c = i16, al = c & 1;
    const int sa = 256 * q4 + 8 * g + 32 * (c >> 1) + 4 * al;
    v2u xt0, xt1;
    {
      const int cp = w >> 1;
      const GAS char* xs = (const GAS char*)p.ws + OFF_XR + (2 * cj + (cp >> 1)) * (S * 2) + 2 * (1 - (cp & 1)) + (w & 1) * 2048 + lane * 16;
      dma2s(xs, LB + B_XC + cp * B_XCS + (w & 1) * 2048);
    }
    asm volatile("global_load_dwordx2 %0, %1, off sc1" : "=v"(xt0) : "v"((const GAS char*)p.ws + OFF_XT + (bch * S + sa) * 2) : "memory");
    asm volatile("global_load_dwordx2 %0, %1, off offset:2048 sc1" : "=v"(xt1) : "v"((const GAS char*)p.ws + OFF_XT + (bch * S + sa) * 2) : "memory");
    dma2s((const GAS char*)p.ws + OFF_ST + w * 2048 + lane * 16, LB + B_ST + w * 2048);
    const float dch = bflo(dv);
    if (lane < 32) *(LAS u16*)(L + B_HC + w * B_HCS + 2 * lane) = ((lane & 31) + 2 * q4 - 30) >= 0 ? (u16)hval : (u16)0;
    if (w >= 4) {
      const int w4 = w - 4, wb = bid < 224 ? bid : 0;
#pragma unroll
      for (int cc = 0; cc < 2; ++cc) {
        const int wr = 16 * cc + (lane >> 2);
        dma1((const GAS char*)p.w1 + (unsigned)((32 * (wb / 14) + wr) * F + 128 * (wb % 14) + 32 * w4 + 8 * (lane & 3)) * 2, LB + A_W1 + w4 * 2048 + cc * 1024);
        dma1((const GAS char*)p.w2 + (unsigned)((32 * (wb >> 2) + wr) * D + 128 * (wb & 3) + 32 * w4 + 8 * (lane & 3)) * 2, LB + A_W2 + w4 * 2048 + cc * 1024);
      }
      WAIT_V(8);
    } else {
      WAIT_V(4);
    }
    if ((w & 3) == 1 && lane == 0) *(LAS u16*)(L + B_XC + (w >> 1) * B_XCS + 2 * (S - 1)) = 0;
    BARRIER();

    LAS unsigned char* ab = L + B_HC + (chl * 4 + (c & 3)) * B_HCS + 2 * (224 + 8 * (c >> 2) + 8 * g) - 128 * q4;
    LAS unsigned char* bb = L + B_XC + (chl * 2 + al) * B_XCS + 2 * (2240 - 1792 - 32 * (c >> 1) + 8 * g) - 128 * q4;
    LAS f32x4* red = (LAS f32x4*)(L + B_RED);
    auto bfinish = [&](int hf, v2u xt) {
      LAS f32x4* rp = red + (chl * 32 + hf * 4 + q4) * 64 + lane;
      const f32x4 a0 = rp[0], a1 = rp[8 * 64], a2 = rp[16 * 64], a3 = rp[24 * 64];
      const int s = sa + 1024 * hf;
      const v2u kvr = *(LAS v2u*)(L + B_XC + (chl * 2 + 1) * B_XCS + 2 * (S - 4 - s));
      v4u g0 = *(LAS v4u*)(L + B_ST + 8 * s), g1 = *(LAS v4u*)(L + B_ST + 8 * s + 16);
      if (__builtin_amdgcn_ballot_w64(g0.y == tag && g0.w == tag && g1.y == tag && g1.w == tag) != ~0ull) {
        for (int spins = 0; spins < SPIN_CAP; ++spins) {
          unsigned long long e[4];
#pragma unroll
          for (int i = 0; i < 4; ++i) e[i] = __hip_atomic_load((const GAS unsigned long long*)(p.ws + OFF_ST + 8 * s) + i, __ATOMIC_RELAXED, __HIP_MEMORY_SCOPE_AGENT);
          g0 = v4u{(unsigned)e[0], (unsigned)(e[0] >> 32), (unsigned)e[1], (unsigned)(e[1] >> 32)};
          g1 = v4u{(unsigned)e[2], (unsigned)(e[2] >> 32), (unsigned)e[3], (unsigned)(e[3] >> 32)};
          if (__builtin_amdgcn_ballot_w64(g0.y == tag && g0.w == tag && g1.y == tag && g1.w == tag) == ~0ull) break;
          __builtin_amdgcn_s_sleep(2);
        }
      }
      const unsigned u0 = g0.x, u1 = g0.z, u2 = g1.x, u3 = g1.z;
      const f32x2 mr01 = {bflo(u0), bflo(u1)}, rr01 = {bfhi(u0), bfhi(u1)}, mr23 = {bflo(u2), bflo(u3)}, rr23 = {bfhi(u2), bfhi(u3)};
      const f32x2 ylo = (a0.xy + a1.xy) + (a2.xy + a3.xy), yhi = (a0.zw + a1.zw) + (a2.zw + a3.zw);
      const float s0_ = al ? ylo.x : yhi.x, s1_ = al ? ylo.y : yhi.y;
      const float r0_ = asf(__builtin_amdgcn_update_dpp(0, asu(s0_), 0xB1, 0xf, 0xf, true));
      const float r1_ = asf(__builtin_amdgcn_update_dpp(0, asu(s1_), 0xB1, 0xf, 0xf, true));
      const f32x2 y01 = {al ? r0_ : ylo.x, al ? yhi.x : r0_}, y23 = {al ? r1_ : ylo.y, al ? yhi.y : r1_};
      const unsigned k1 = kvr.y, k0 = kvr.x;
      const f32x2 x01 = up2(xt.x), x23 = up2(xt.y);
      const f32x2 dd = {dch, dch};
      const f32x2 t01 = fma2(f32x2{bfhi(k1), bflo(k1)}, dd, y01), t23 = fma2(f32x2{bfhi(k0), bflo(k0)}, dd, y23);
      const f32x2 h01 = fma2((x01 - mr01) * rr01, t01, x01), h23 = fma2((x23 - mr23) * rr23, t23, x23);
      const int sl = (s & 63) >> 2;
      __builtin_amdgcn_raw_buffer_store_b64(v2u{pk(h01.x, h01.y), pk(h23.x, h23.y)}, rs,
                                            OFF_HT + ((s >> 6) * D + bch) * 128 + ((sl ^ (((bch >> 1) & 1) << 3)) << 3), 0, SC1);
    };
    {
      v4u af[8], bf[8], ag[8], bg[8];
      f32x4 acc[4];
#pragma unroll
      for (int i = 0; i < 4; ++i) acc[i] = f32x4{0.f, 0.f, 0.f, 0.f};
#pragma unroll
      for (int i = 0; i < 4; ++i) { bf[i] = *(LAS v4u*)(bb + 512 * (7 - i)); af[i] = *(LAS v4u*)(ab + 512 * i); }
#pragma unroll
      for (int i = 0; i < 4; ++i) { bg[i] = *(LAS v4u*)(bb - 64 + 512 * (7 - i)); ag[i] = *(LAS v4u*)(ab - 64 + 512 * i); }
      SCHED();
#pragma unroll
      for (int ip = 0; ip < 4; ++ip)
#pragma unroll
        for (int i = ip; i < 4; ++i) acc[i] = mfma16(af[i - ip], bf[ip], acc[i]);
#pragma unroll
      for (int i = 4; i < 8; ++i) { bf[i] = *(LAS v4u*)(bb + 512 * (7 - i)); af[i] = *(LAS v4u*)(ab + 512 * i); }
      SCHED();

#pragma unroll
      for (int i = 0; i < 4; ++i)
#pragma unroll
        for (int ip = 0; ip <= i; ++ip) acc[i] = mfma16(ag[i - ip], bg[ip], acc[i]);
      SCHED();
      asm volatile("s_waitcnt vmcnt(0)" : "+v"(xt0), "+v"(xt1) :: "memory");
      if (w >= 4 && bid < 224) {
#pragma unroll
        for (int cc = 0; cc < 2; ++cc) {
          const int v = w & 3;
          const int off = v * 2048 + (16 * cc + 8 * hh + tq) * 64 + 32 * blk + 16 * (tp & 1) + 8 * (tp >> 1);
          const v4u f1 = trfrag(L + A_W1 + off, 256);
          const v4u f2 = trfrag(L + A_W2 + off, 256);
          const int k16 = 2 * (bid / 14) + cc, nt = bid % 14;
          st16(f1, rs, OFF_W1 + (((nt * 8 + (k16 >> 4) * 4 + v) * 16) + (k16 & 15)) * 1024 + lane * 16);
          const int ka = 2 * (bid >> 2) + cc, dt = 2 * (bid & 3) + (v >> 1);
          st16(f2, rs, OFF_W2 + (((dt * 8 + ka / 14) * 28) + (ka % 14) * 2 + (v & 1)) * 1024 + lane * 16);
        }
      }
#pragma unroll
      for (int i = 0; i < 4; ++i) red[(w * 8 + i) * 64 + lane] = acc[i];
      BARRIER();
      f32x4 ac2[4];
#pragma unroll
      for (int i = 0; i < 4; ++i) ac2[i] = f32x4{0.f, 0.f, 0.f, 0.f};
      bfinish(0, xt0);
      SCHED();
#pragma unroll
      for (int ip = 0; ip < 8; ++ip) {
#pragma unroll
        for (int i = (ip > 4 ? ip : 4); i < 8; ++i) ac2[i - 4] = mfma16(af[i - ip], bf[ip], ac2[i - 4]);
        SCHED();
        if (ip < 4) { ag[4 + ip] = *(LAS v4u*)(ab - 64 + 512 * (4 + ip)); bg[4 + ip] = *(LAS v4u*)(bb - 64 + 512 * (7 - 4 - ip)); }
        SCHED();
        if (ip == 4) {
          WAIT_V(0);
          lds_count_flag8(L, 4, lane, tag, rs, OFF_FB0 + bid * 16);
          asm volatile("global_load_ushort %0, %1, %2" : "=v"(b1n) : "v"((unsigned)(128 * d_nt + 32 * nq + (lane & 31)) * 2), "s"(p.b1) : "memory");
          SCHED();
        }
      }
      SCHED();
#pragma unroll
      for (int i = 4; i < 8; ++i) {
#pragma unroll
        for (int ip = 0; ip <= i; ++ip) {
          ac2[i - 4] = mfma16(ag[i - ip], bg[ip], ac2[i - 4]);
        }
        SCHED();
        if (i > 4) red[(w * 8 + 4 + i - 5) * 64 + lane] = ac2[i - 5];
        SCHED();
      }
      red[(w * 8 + 7) * 64 + lane] = ac2[3];
    }
    BARRIER();
    if (w == 0) LOOK_ISSUE(OFF_FB0 + xcd * 4096 + lane * 16);
    bfinish(1, xt1);
    if (w == 0) LOOK_CHECK(1, OFF_FB0 + xcd * 4096 + lane * 16);
  }

  {
    BARRIER();
    const GAS unsigned char* tsrc = (const GAS unsigned char*)p.ws + OFF_HT + (size_t)d_mt * 65536 + w * 8192 + lane * 16;
    const unsigned w2off = OFF_W2 + (e_dt * 8 + w) * 28 * 1024 + lane * 16;
    auto second_wait = [&]() {
      if (w == 0) { poll4(rs, OFF_FB1 + xcd * 4096 + lane * 16, tag); lds_set(L, 0); }
      lds_wait(L, 0);
    };
    auto second_res = [&]() {
      const int rr = 16 * (w & 3) + (lane >> 2);
      const int dch2 = 64 * e_dt + rr;
      dma1s((const GAS unsigned char*)p.ws + OFF_HT + ((16 * (w >> 2) + e_mt) * D + dch2) * 128 + (((unsigned)e_j ^ ((dch2 >> 1) & 1)) << 6) + 16 * (lane & 3),
            LB + E_RES + w * 1024);
    };
    if (d_act) { dma4s(tsrc, LB + D_TILE + w * 8192); dma4s(tsrc + 4096, LB + D_TILE + w * 8192 + 4096); }
    if (d_act) { WAIT_V(8); } else { WAIT_V(0); }
    lds_count_flag8(L, 5, lane, tag, rs, OFF_FB1 + bid * 16);
    if (d_act) {
      asm volatile("" : "+v"(b1n) :: "memory");
#pragma unroll
      for (int i = 0; i < 8; ++i) w1load(i);
      SCHED();
      const unsigned basel = (unsigned)(D_TILE + (256 * kh + 8 * hh + tq) * 128 + 32 * blk + 8 * tp);
      const unsigned fsw = ((tq >> 1) & 1) << 6;
      const unsigned adr[2] = {basel + ((unsigned)(kh << 6) ^ fsw), basel + ((unsigned)((kh ^ 1) << 6) ^ fsw)};
      f32x16 acc[2][2];
      auto fraddr = [&](int c, int m) { return L + (adr[m & 1] + (unsigned)(c * 65536 + 8192 * ((((m >> 1) >> 2) + nq) & 3)) + 2048 * ((m >> 1) & 3)); };
      float xg[16];
      unsigned og[8];
      auto hstore = [&](int c) {
        const unsigned ho = OFF_HF + ((32 * c + 2 * d_mt + kh) * 112 + 8 * d_nt + 2 * nq) * 1024 + lane * 16;
        st16(v4u{og[0], og[1], og[2], og[3]}, rs, ho);
        st16(v4u{og[4], og[5], og[6], og[7]}, rs, ho + 1024);
      };
      v2u sg[2][2];
      auto sload = [&](int c) {
        const GAS char* sp = (const GAS char*)p.ws + OFF_HS + (1024 * c + 64 * d_mt + 32 * kh + (lane & 31)) * 16;
        asm volatile("global_load_dwordx2 %0, %1, off sc1" : "=v"(sg[c][0]) : "v"(sp) : "memory");
        asm volatile("global_load_dwordx2 %0, %1, off offset:8 sc1" : "=v"(sg[c][1]) : "v"(sp) : "memory");
      };
      auto mstep = [&](int c) {
#pragma unroll
        for (int r = 0; r < 2; ++r)
#pragma unroll
          for (int e = 0; e < 16; ++e) acc[c][r][e] = 0.f;
        v4u fr[4];
#pragma unroll
        for (int n = 0; n < 3; ++n) fr[n] = trfrag(fraddr(c, n), 512);
        SCHED();
#pragma unroll
        for (int n = 0; n < 32; ++n) {
          const int i = n >> 1, r = n & 1;
          if (c == 0 && r == 0) {
            if (n < 16) { w1load(8 + i); WAITF(8, wf1[i]); }
            if (n == 16) {
              WAITF(7, wf1[8]);
              if (w == 0) { LOOK_CHECK(4, OFF_FB1 + xcd * 4096 + lane * 16); lds_set(L, 0); }
              lds_wait(L, 0);
            }
            if (n == 18) WAITF(7, wf1[9]);
            if (n == 20) WAITF(7, wf1[10]);
            if (n == 22) WAITF(7, wf1[11]);
            if (n == 24) WAITF(7, wf1[12]);
            if (n == 26) WAITF(7, wf1[13]);
            if (n == 28) WAITF(7, wf1[14]);
            if (n == 30) WAITF(9, wf1[15]);
          }
          if (c == 0 && n == 7 && w == 0) LOOK_ISSUE(OFF_FB1 + xcd * 4096 + lane * 16);
          if (c == 0 && n >= 16 && (n & 1) == 0) dma1s(tsrc + 16 * 65536 + ((n - 16) >> 1) * 1024, LB + D_TILE + 65536 + w * 8192 + ((n - 16) >> 1) * 1024);
          if (c == 1 && n == 1) second_res();
          if (c == 1 && n == 15) sload(1);
          if (c == 0 && n == 29) {
            sload(0);
            if (w == 0) dma1s((const GAS char*)p.ws + OFF_CS + d_nt * 1024 + lane * 16, LB + D_CSG);
          }
          if (n + 3 < 32) fr[(n + 3) & 3] = trfrag(fraddr(c, n + 3), 512);
          acc[c][r] = mfma32(wf1[i], fr[n & 3], acc[c][r]);
          if (c == 1 && (n & 1) == 0) wf2[n >> 1] = ld16(rs, w2off + (n >> 1) * 1024);
          SCHED();
        }
      };
      auto xwrite = [&](int c) {
        LAS f32x4* ex = (LAS f32x4*)(L + D_TILE + c * 32768) + w * 256 + lane;
#pragma unroll
        for (int qd = 0; qd < 4; ++qd) ex[qd * 64] = f32x4{acc[c][1][4 * qd], acc[c][1][4 * qd + 1], acc[c][1][4 * qd + 2], acc[c][1][4 * qd + 3]};
      };
      auto xpre = [&](int c) {
        LAS f32x4* ex = (LAS f32x4*)(L + D_TILE + c * 32768) + (w ^ 4) * 256 + lane;
        LAS f32x4* cb = (LAS f32x4*)(L + D_CB) + 16 * nq + 4 * hh;
        f32x4 pp[4], c0[4], c1[4];
#pragma unroll
        for (int qd = 0; qd < 4; ++qd) { pp[qd] = ex[qd * 64]; c0[qd] = cb[8 * (qd >> 1) + 2 * (qd & 1)]; c1[qd] = cb[8 * (qd >> 1) + 2 * (qd & 1) + 1]; }
        SCHED();
        unsigned u0 = sg[c][0].x, u1 = sg[c][1].x;
        if (__builtin_amdgcn_ballot_w64(((sg[c][0].y ^ tag) | (sg[c][1].y ^ tag)) != 0u) != 0ull) {
          const GAS unsigned long long* sp = (const GAS unsigned long long*)(p.ws + OFF_HS + (1024 * c + 64 * d_mt + 32 * kh + (lane & 31)) * 16);
          for (int spins = 0; spins < SPIN_CAP; ++spins) {
            const unsigned long long e0 = __hip_atomic_load(sp, __ATOMIC_RELAXED, __HIP_MEMORY_SCOPE_AGENT), e1 = __hip_atomic_load(sp + 1, __ATOMIC_RELAXED, __HIP_MEMORY_SCOPE_AGENT);
            u0 = (unsigned)e0; u1 = (unsigned)e1;
            if (__builtin_amdgcn_ballot_w64((((unsigned)(e0 >> 32) ^ tag) | ((unsigned)(e1 >> 32) ^ tag)) != 0u) == 0ull) break;
            __builtin_amdgcn_s_sleep(2);
          }
        }
        const float rstd = asf(u0), nm = asf(u1);
        const f32x2 rs2 = {rstd, rstd}, nm2 = {nm, nm};
#pragma unroll
        for (int qd = 0; qd < 4; ++qd)
#pragma unroll
          for (int hf = 0; hf < 2; ++hf) {
            const int e = 2 * qd + hf;
            const f32x4 cc = hf ? c1[qd] : c0[qd];
            const f32x2 a = f32x2{acc[c][0][2 * e], acc[c][0][2 * e + 1]} + (hf ? pp[qd].zw : pp[qd].xy);
            const f32x2 x = fma2(rs2, a, fma2(nm2, cc.xz, cc.yw));
            xg[2 * e] = x.x; xg[2 * e + 1] = x.y;
          }
      };
      auto cbtable = [&]() {
        const int n = 32 * nq + (lane & 31);
        v2u gq = *(LAS v2u*)(L + D_CSG + 8 * n);
        unsigned cs = gq.x;
        if (__builtin_amdgcn_ballot_w64(gq.y != tag) != 0ull) {
          for (int spins = 0; spins < SPIN_CAP; ++spins) {
            const unsigned long long e0 = __hip_atomic_load((const GAS unsigned long long*)(p.ws + OFF_CS) + 128 * d_nt + n, __ATOMIC_RELAXED, __HIP_MEMORY_SCOPE_AGENT);
            cs = (unsigned)e0;
            if (__builtin_amdgcn_ballot_w64((unsigned)(e0 >> 32) != tag) == 0ull) break;
            __builtin_amdgcn_s_sleep(2);
          }
        }
        *(LAS f32x2*)(L + D_CB + 8 * n) = f32x2{asf(cs), bflo(b1n)};
        asm volatile("s_waitcnt lgkmcnt(0)" ::: "memory");
      };
      auto gelu_all = [&]() {
#pragma unroll
        for (int e = 0; e < 8; ++e) {
          const f32x2 x = {xg[2 * e], xg[2 * e + 1]};
          const f32x2 pz = fma2(x * x, f32x2{-0.10294324f, -0.10294324f}, f32x2{-2.3022082f, -2.3022082f});
          const f32x2 z = x * pz;
          const f32x2 den = f32x2{__builtin_amdgcn_exp2f(z.x), __builtin_amdgcn_exp2f(z.y)} + f32x2{1.f, 1.f};
          const f32x2 y = x * f32x2{__builtin_amdgcn_rcpf(den.x), __builtin_amdgcn_rcpf(den.y)};
          og[e] = pk(y.x, y.y);
        }
      };
      WAIT_V(8);
      BARRIER();
      SCHED();
      mstep(0);
      BARRIER();
      xwrite(0);
      asm volatile("s_waitcnt vmcnt(0)" : "+v"(sg[0][0]), "+v"(sg[0][1]) :: "memory");
      BARRIER();
      cbtable();
      xpre(0);
      gelu_all();
      hstore(0);
      BARRIER();
      mstep(1);
      xwrite(1);
      asm volatile("s_waitcnt vmcnt(8)" : "+v"(sg[1][0]), "+v"(sg[1][1]) :: "memory");
      lds_count_flag(L, 2, 8, tid, d_act, tag, rs, OFF_FD + d_mt * 256 + d_nt * 16);
      BARRIER();
      xpre(1);
      gelu_all();
      hstore(1);
    }
    asm volatile("" ::: "memory");
    if (!d_act) {
      const int u = xcd * 4 + (bix - 28);
      if (u >= 16) {
        const int kg = tid >> 4, cc = tid & 15;
        f32x2 cs[4] = {{0.f, 0.f}, {0.f, 0.f}, {0.f, 0.f}, {0.f, 0.f}};
        if (cc < 14) {
          v4u wv[16];
#pragma unroll
          for (int i = 0; i < 16; ++i) wv[i] = *(const GAS v4u*)((const GAS char*)p.w1 + (unsigned)((16 * kg + i) * F + 112 * (u - 16) + 8 * cc) * 2);
#pragma unroll
          for (int i = 0; i < 16; ++i) { cs[0] += up2(wv[i].x); cs[1] += up2(wv[i].y); cs[2] += up2(wv[i].z); cs[3] += up2(wv[i].w); }
        }
        LAS f32x4* t = (LAS f32x4*)(L + 65536) + (kg * 16 + cc) * 2;
        t[0] = f32x4{cs[0].x, cs[0].y, cs[1].x, cs[1].y}; t[1] = f32x4{cs[2].x, cs[2].y, cs[3].x, cs[3].y};
        BARRIER();
        if (tid < 112) {
          float a = 0.f;
#pragma unroll
          for (int i = 0; i < 32; ++i) a += *(LAS float*)(L + 65536 + (i * 128 + tid) * 4);
          __builtin_amdgcn_raw_buffer_store_b64(v2u{asu(a), tag}, rs, OFF_CS + 8 * (112 * (u - 16) + tid), 0, SC1);
        }
      }
      if (u >= 16) second_wait();
      {
        const GAS unsigned char* src = (const GAS unsigned char*)p.ws + OFF_HT + (size_t)u * 65536 + w * 8192 + lane * 16;
        dma4s(src, LB + w * 8192); dma4s(src + 4096, LB + w * 8192 + 4096);
      }
      WAIT_V(0);
      {
        float s1[2][2] = {{0.f, 0.f}, {0.f, 0.f}}, s2[2][2] = {{0.f, 0.f}, {0.f, 0.f}};
        LAS unsigned char* sp = L + w * 8192 + (8 * hh + tq) * 128 + 32 * blk + 8 * tp;
        const unsigned fsw = ((tq >> 1) & 1) << 6;
#pragma unroll
        for (int n = 0; n < 8; ++n) {
          const int r = n & 1;
          const v4u hf = trfrag(sp + (n >> 1) * 2048 + (((unsigned)r << 6) ^ fsw), 512);
          s1[r][0] = dot2(hf.x, ONES, s1[r][0]); s2[r][0] = dot2(hf.x, hf.x, s2[r][0]);
          s1[r][1] = dot2(hf.y, ONES, s1[r][1]); s2[r][1] = dot2(hf.y, hf.y, s2[r][1]);
          s1[r][0] = dot2(hf.z, ONES, s1[r][0]); s2[r][0] = dot2(hf.z, hf.z, s2[r][0]);
          s1[r][1] = dot2(hf.w, ONES, s1[r][1]); s2[r][1] = dot2(hf.w, hf.w, s2[r][1]);
        }
#pragma unroll
        for (int r = 0; r < 2; ++r) {
          const float a = halfsum(s1[r][0] + s1[r][1]), b = halfsum(s2[r][0] + s2[r][1]);
          if (lane < 32) *(LAS f32x2*)(L + 98304 + (w * 64 + 32 * r + lane) * 8) = f32x2{a, b};
        }
      }
      BARRIER();
      if (w == 0) {
        f32x2 sa = {0.f, 0.f};
#pragma unroll
        for (int ww = 0; ww < 8; ++ww) sa += *(LAS f32x2*)(L + 98304 + (ww * 64 + lane) * 8);
        const float mean = sa.x * (1.0f / D);
        float var = __builtin_fmaf(-mean, mean, sa.y * (1.0f / D));
        var = var > 0.f ? var : 0.f;
        const float r1 = __builtin_amdgcn_rsqf(var + 1e-5f);
        const float rstd = r1 * __builtin_amdgcn_rsqf(__builtin_fmaf(var * r1, r1, 1e-5f));
        __builtin_amdgcn_raw_buffer_store_b64(v2u{asu(rstd), tag}, rs, OFF_HS + (64 * u + lane) * 16, 0, SC1);
        __builtin_amdgcn_raw_buffer_store_b64(v2u{asu(-rstd * mean), tag}, rs, OFF_HS + (64 * u + lane) * 16 + 8, 0, SC1);
      }
      if (u < 16) second_wait();
      second_res();
#pragma unroll
      for (int i = 0; i < 16; ++i) wf2[i] = ld16(rs, w2off + i * 1024);
    }
  }

  {
    const int sbo = w >> 2, dbo = (w >> 1) & 1, hf = w & 1;
    if (w == 0) poll1(rs, OFF_FD + e_mt * 256 + (lane < 14 ? lane : 0) * 16, tag);
    const unsigned w2off = OFF_W2 + (e_dt * 8 + w) * 28 * 1024 + lane * 16;
#pragma unroll
    for (int i = 16; i < 28; ++i) wf2[i] = ld16(rs, w2off + i * 1024);
    const v4u b2v = *(const GAS v4u*)((const GAS char*)p.b2 + (unsigned)(64 * e_dt + 32 * dbo + 16 * hf + 8 * hh) * 2);
    SCHED();
    WAIT_V(13);
    BARRIER();
    if (d_act && tid == 0) st4(tag, rs, OFF_FD + 4096 + d_mt * 256 + d_nt * 16);
    const unsigned hoff0 = OFF_HF + ((2 * e_mt + e_j) * 112 + 14 * w) * 1024 + lane * 16, hoff1 = hoff0 + 32 * 112 * 1024;
    if (w == 0) { poll1(rs, OFF_FD + 4096 + e_mt * 256 + (lane < 14 ? lane : 0) * 16, tag); lds_set(L, 1); }
    v4u hf0[14], hf1[14];
#pragma unroll
    for (int k = 0; k < 14; ++k) hf0[k] = ld16(rs, hoff0 + k * 1024);
    SCHED();
    lds_wait(L, 1);
    LAS f32x4* red = (LAS f32x4*)(L + E_RED + w * 16384) + lane;
    {
      f32x16 acc[2];
#pragma unroll
      for (int e = 0; e < 16; ++e) { acc[0][e] = 0.f; acc[1][e] = 0.f; }
#pragma unroll
      for (int k = 0; k < 14; ++k) {
        acc[0] = mfma32(wf2[2 * k], hf0[k], acc[0]); acc[1] = mfma32(wf2[2 * k + 1], hf0[k], acc[1]);
        SCHED();
        hf1[k] = ld16(rs, hoff1 + k * 1024);
        SCHED();
      }
#pragma unroll
      for (int d = 0; d < 2; ++d)
#pragma unroll
        for (int qd = 0; qd < 4; ++qd) red[(d * 4 + qd) * 64] = f32x4{acc[d][4 * qd], acc[d][4 * qd + 1], acc[d][4 * qd + 2], acc[d][4 * qd + 3]};
    }
    {
      f32x16 acc[2];
#pragma unroll
      for (int e = 0; e < 16; ++e) { acc[0][e] = 0.f; acc[1][e] = 0.f; }
#pragma unroll
      for (int k = 0; k < 14; ++k) { acc[0] = mfma32(wf2[2 * k], hf1[k], acc[0]); acc[1] = mfma32(wf2[2 * k + 1], hf1[k], acc[1]); }
#pragma unroll
      for (int d = 0; d < 2; ++d)
#pragma unroll
        for (int qd = 0; qd < 4; ++qd) red[512 + (d * 4 + qd) * 64] = f32x4{acc[d][4 * qd], acc[d][4 * qd + 1], acc[d][4 * qd + 2], acc[d][4 * qd + 3]};
    }
    BARRIER();
    f32x2 o[4] = {{0.f, 0.f}, {0.f, 0.f}, {0.f, 0.f}, {0.f, 0.f}};
#pragma unroll
    for (int ww = 0; ww < 8; ++ww) {
      LAS f32x4* rp = (LAS f32x4*)(L + E_RED + ww * 16384) + sbo * 512 + (dbo * 4 + 2 * hf) * 64 + lane;
      const f32x4 a = rp[0], b = rp[64];
      o[0] += a.xy; o[1] += a.zw; o[2] += b.xy; o[3] += b.zw;
    }
    const v4u hr = trfrag(L + E_RES + sbo * 4096 + (32 * dbo + 16 * hf + 8 * hh + tq) * 64 + 32 * blk + 8 * tp, 256);
    o[0] += up2(b2v.x) + up2(hr.x); o[1] += up2(b2v.y) + up2(hr.y);
    o[2] += up2(b2v.z) + up2(hr.z); o[3] += up2(b2v.w) + up2(hr.w);
    const v4u ov = {pk(o[0].x, o[0].y), pk(o[1].x, o[1].y), pk(o[2].x, o[2].y), pk(o[3].x, o[3].y)};
    {
      const rsrc_t ro = __builtin_amdgcn_make_buffer_rsrc((void*)p.out, 0, S * D * 2, 0x00020000);
      __builtin_amdgcn_raw_buffer_store_b128(ov, ro, (unsigned)((1024 * sbo + 64 * e_mt + 32 * e_j + (lane & 31)) * D + 64 * e_dt + 32 * dbo + 16 * hf + 8 * hh) * 2, 0, 0);
    }
    if (bid == 0 && tid == 0) st4(tag, rs, OFF_EP);
  }
}

extern "C" void kernel_launch(void* const* d_in, const int* in_sizes, int n_in, void* d_out, int out_size, void* d_ws,
                              size_t ws_size, hipStream_t stream) {
  Params p;
  p.q = (const u16*)d_in[0]; p.k = (const u16*)d_in[1]; p.v = (const u16*)d_in[2];
  p.shk = (const u16*)d_in[3]; p.ssk = (const u16*)d_in[4]; p.ssd = (const u16*)d_in[5];
  p.w1 = (const u16*)d_in[7]; p.b1 = (const u16*)d_in[8]; p.w2 = (const u16*)d_in[9]; p.b2 = (const u16*)d_in[10];
  p.out = (u16*)d_out; p.ws = (unsigned char*)d_ws;
  fused<<<256, 512, 0, stream>>>(p);
}
```
